```python
import torch

def _softmax(x, axis=-1):
    return torch.softmax(x, dim=axis)
def _gelu(x):
    return torch.nn.functional.gelu(x, approximate="tanh")
def _silu(x):
    return torch.nn.functional.silu(x)
def _sigmoid(x):
    return torch.sigmoid(x)
def _layernorm(x, axis=-1, eps=1e-5):
    m = x.mean(dim=axis, keepdim=True)
    v = x.var(dim=axis, keepdim=True, unbiased=False)
    return (x - m) / torch.sqrt(v + eps)
def _scatter_add(tgt, idx, src, axis=0):
    return tgt.clone().index_add_(axis, idx, src)
def _segment_sum(src, seg, n):
    out = torch.zeros((n,) + src.shape[1:], dtype=src.dtype)
    return out.index_add_(0, seg, src)
def _pad(x, pw):
    flat = tuple(v for pair in reversed(pw) for v in pair)
    return torch.nn.functional.pad(x, flat)

kernel_name = 'sequential_preLN__spectral_fnet__moe_arch_mod__prompt'

def setup_inputs():

    _w = (lambda a: (lambda t: t.to(torch.bfloat16) if t.is_floating_point() else t)(torch.as_tensor(a)))
    def _A_setup():
        B, S, D = 1, 2048, 448
        ns = torch.arange(S)[:, None] * torch.arange(S)[None, :]
        nd = torch.arange(D)[:, None] * torch.arange(D)[None, :]
        Ws_re = torch.cos(-2.0 * torch.pi * ns / S).to(float)
        Ws_im = torch.sin(-2.0 * torch.pi * ns / S).to(float)
        Wd_re = torch.cos(-2.0 * torch.pi * nd / D).to(float)
        Wd_im = torch.sin(-2.0 * torch.pi * nd / D).to(float)
        return {
            "x": _w((lambda *s: torch.randn(*s, dtype=torch.bfloat16) * 0.05)(B, S, D)),
            "dft_seq_re": _w(Ws_re),
            "dft_seq_im": _w(Ws_im),
            "dft_hid_re": _w(Wd_re),
            "dft_hid_im": _w(Wd_im),
        }
    _A_in = _A_setup()

    def _B_setup():
        B, S, D, F, C = 1, 2048, 448, 1792, 8
        return {
            "x": _w((lambda *s: torch.randn(*s, dtype=torch.bfloat16) * 0.05)(B, S, D)),
            "w_r": _w((lambda *s: torch.randn(*s, dtype=torch.bfloat16) * 1.6)(D)),
            "w_gate": _w((lambda *s: torch.randn(*s, dtype=torch.bfloat16) * 0.4)(D, F)),
            "w_up": _w((lambda *s: torch.randn(*s, dtype=torch.bfloat16) * 0.4)(D, F)),
            "w_down": _w((lambda *s: torch.randn(*s, dtype=torch.bfloat16) * 0.4)(F, D)),
        }
    _B_in = _B_setup()

    return {
        "A_x": _A_in["x"],
        "A_dft_seq_re": _A_in["dft_seq_re"],
        "A_dft_seq_im": _A_in["dft_seq_im"],
        "A_dft_hid_re": _A_in["dft_hid_re"],
        "A_dft_hid_im": _A_in["dft_hid_im"],
        "B_x": _B_in["x"],
        "B_w_r": _B_in["w_r"],
        "B_w_gate": _B_in["w_gate"],
        "B_w_up": _B_in["w_up"],
        "B_w_down": _B_in["w_down"],
    }

def reference(A_x, A_dft_seq_re, A_dft_seq_im, A_dft_hid_re, A_dft_hid_im, B_x, B_w_r, B_w_gate, B_w_up, B_w_down):
    def _A_ref(x, dft_seq_re, dft_seq_im, dft_hid_re, dft_hid_im):
        a = torch.einsum("bsd,ed->bse", x, dft_hid_re)
        b = torch.einsum("bsd,ed->bse", x, dft_hid_im)
        y = torch.einsum("ts,bse->bte", dft_seq_re, a) - torch.einsum("ts,bse->bte", dft_seq_im, b)
        return y

    def _B_ref(x, w_r, w_gate, w_up, w_down):
        B, S, D = x.shape
        C = 8
        r = x @ w_r
        idx = torch.argsort(-r, axis=-1)[:, :C]
        idx_e = (lambda x, n, axis: torch.repeat_interleave(x, n, dim=axis))(idx.reshape(B, C, 1), D, axis=2)
        x_sel = (lambda x, i, axis: torch.take_along_dim(x, i, dim=axis))(x, idx_e, axis=1)
        g = _silu(x_sel @ w_gate)
        u = x_sel @ w_up
        y = (g * u) @ w_down
        r_sel = _sigmoid((lambda x, i, axis: torch.take_along_dim(x, i, dim=axis))(r, idx, axis=1))
        y = y * r_sel.reshape(B, C, 1)
        b_off = (torch.arange(B) * S).reshape(B, 1)
        flat_idx = (idx + b_off).reshape(B * C)
        zero = (x * 0.0).reshape(B * S, D)
        upd = _scatter_add(zero, flat_idx, y.reshape(B * C, D), axis=0)
        return x + upd.reshape(B, S, D)

    x = A_x
    h = x + _A_ref(_layernorm(x, axis=-1), A_dft_seq_re, A_dft_seq_im, A_dft_hid_re, A_dft_hid_im)
    h2 = h + _B_ref(_layernorm(h, axis=-1), B_w_r, B_w_gate, B_w_up, B_w_down)
    return h2
```

```cpp
#include <hip/hip_runtime.h>
#include <hip/hip_bf16.h>
typedef unsigned short u16;
typedef short bf16x8 __attribute__((ext_vector_type(8)));
typedef float f32x4 __attribute__((ext_vector_type(4)));
typedef unsigned v4u __attribute__((ext_vector_type(4)));
constexpr int S = 2048, D = 448, F = 1792, C = 8;

__device__ __forceinline__ float bf2f(u16 v) { return __uint_as_float((unsigned)v << 16); }
__device__ __forceinline__ u16 f2bf(float f) { __hip_bfloat16 h = __float2bfloat16(f); return *(u16*)&h; }
__device__ __forceinline__ float rbf(float f) { return bf2f(f2bf(f)); }
__device__ __forceinline__ float wsum(float v) { for (int o = 32; o; o >>= 1) v += __shfl_xor(v, o); return v; }

__device__ __forceinline__ void ln8(const float (&x)[8], bool act, float (&y)[8]) {
  float s = 0; for (int j = 0; j < 8; ++j) s += act ? x[j] : 0.f;
  float mean = wsum(s) * (1.f / D);
  float q = 0; for (int j = 0; j < 8; ++j) { float d = x[j] - mean; q += act ? d * d : 0.f; }
  float var = wsum(q) * (1.f / D);
  float m = rbf(mean), v = rbf(var);
  float sd = rbf(sqrtf(rbf(v + 1e-5f)));
  for (int j = 0; j < 8; ++j) y[j] = rbf(rbf(x[j] - m) / sd);
}

__global__ void k_ln(const u16* x, u16* xn) {
  int row = blockIdx.x * 4 + (threadIdx.x >> 6), lane = threadIdx.x & 63; bool act = lane < 56;
  float v[8], y[8];
  for (int j = 0; j < 8; ++j) v[j] = act ? bf2f(x[row * D + lane * 8 + j]) : 0.f;
  ln8(v, act, y);
  if (act) for (int j = 0; j < 8; ++j) xn[row * D + lane * 8 + j] = f2bf(y[j]);
}
__global__ void k_g1(const u16* xn, const u16* wre, const u16* wim, u16* abT) {
  int lane = threadIdx.x & 63, w = threadIdx.x >> 6;
  int e0 = blockIdx.y * 16, s0 = blockIdx.x * 64 + w * 16;
  const u16* W = e0 < D ? wre + e0 * D : wim + (e0 - D) * D;
  f32x4 acc = {0,0,0,0};
  for (int k = 0; k < D; k += 32) {
    bf16x8 a = *(const bf16x8*)(xn + (s0 + (lane & 15)) * D + k + (lane >> 4) * 8);
    bf16x8 b = *(const bf16x8*)(W + (lane & 15) * D + k + (lane >> 4) * 8);
    acc = __builtin_amdgcn_mfma_f32_16x16x32_bf16(a, b, acc, 0, 0, 0);
  }
  for (int j = 0; j < 4; ++j) abT[(e0 + (lane & 15)) * S + s0 + (lane >> 4) * 4 + j] = f2bf(acc[j]);
}
__global__ void k_g2(const u16* cre, const u16* cim, const u16* abT, u16* Y) {
  int lane = threadIdx.x & 63, w = threadIdx.x >> 6;
  int p = blockIdx.z, n0 = blockIdx.y * 112, t0 = blockIdx.x * 64 + w * 16;
  const u16* Cm = p ? cim : cre; const u16* B = abT + (size_t)p * D * S;
  f32x4 acc[7];
  for (int n = 0; n < 7; ++n) acc[n] = (f32x4){0,0,0,0};
  for (int k = 0; k < S; k += 32) {
    bf16x8 a = *(const bf16x8*)(Cm + (size_t)(t0 + (lane & 15)) * S + k + (lane >> 4) * 8);
    #pragma unroll
    for (int n = 0; n < 7; ++n) {
      bf16x8 b = *(const bf16x8*)(B + (size_t)(n0 + n * 16 + (lane & 15)) * S + k + (lane >> 4) * 8);
      acc[n] = __builtin_amdgcn_mfma_f32_16x16x32_bf16(b, a, acc[n], 0, 0, 0);
    }
  }
  for (int n = 0; n < 7; ++n) for (int j = 0; j < 4; ++j)
    Y[((size_t)p * S + t0 + (lane & 15)) * D + n0 + n * 16 + (lane >> 4) * 4 + j] = f2bf(acc[n][j]);
}
__global__ void k_row(const u16* x, const u16* Y, const u16* wr, u16* hbuf, u16* hnbuf, u16* rbuf, u16* out) {
  int row = blockIdx.x * 4 + (threadIdx.x >> 6), lane = threadIdx.x & 63; bool act = lane < 56;
  float h[8], hn[8];
  for (int j = 0; j < 8; ++j) {
    int e = lane * 8 + j;
    h[j] = act ? rbf(bf2f(x[row * D + e]) + rbf(bf2f(Y[row * D + e]) - bf2f(Y[(size_t)(S + row) * D + e]))) : 0.f;
  }
  ln8(h, act, hn);
  float r = 0; for (int j = 0; j < 8; ++j) r += act ? hn[j] * bf2f(wr[lane * 8 + j]) : 0.f;
  r = wsum(r);
  if (lane == 0) rbuf[row] = f2bf(r);
  if (act) for (int j = 0; j < 8; ++j) { int e = row * D + lane * 8 + j; hbuf[e] = f2bf(h[j]); hnbuf[e] = f2bf(hn[j]); out[e] = f2bf(h[j] + hn[j]); }
}
__device__ __forceinline__ unsigned okey(u16 v, int idx) { unsigned u = v; u = (u & 0x8000) ? (~u & 0xffff) : (u | 0x8000); return (u << 16) | (unsigned)(2047 - idx); }
__global__ void k_gu(const u16* rbuf, const u16* hnbuf, const u16* wg, const u16* wu, u16* gu, int* idxout) {
  __shared__ unsigned keys[S]; __shared__ int sel[C]; __shared__ float xs[C][D]; __shared__ float red[2][C][16][16];
  int tid = threadIdx.x;
  for (int i = tid; i < S; i += 256) keys[i] = okey(rbuf[i], i);
  __syncthreads();
  for (int i = tid; i < S; i += 256) { unsigned k = keys[i]; int c = 0; for (int j = 0; j < S; ++j) c += keys[j] > k; if (c < C) sel[c] = i; }
  __syncthreads();
  if (blockIdx.x == 0 && tid < C) idxout[tid] = sel[tid];
  for (int i = tid; i < C * D; i += 256) xs[i / D][i % D] = bf2f(hnbuf[sel[i / D] * D + i % D]);
  __syncthreads();
  int f = blockIdx.x * 16 + (tid & 15), kq = tid >> 4;
  float g[C], u[C]; for (int c = 0; c < C; ++c) g[c] = u[c] = 0;
  for (int d = kq * 28; d < kq * 28 + 28; ++d) { float a = bf2f(wg[d * F + f]), b = bf2f(wu[d * F + f]); for (int c = 0; c < C; ++c) { g[c] += xs[c][d] * a; u[c] += xs[c][d] * b; } }
  for (int c = 0; c < C; ++c) { red[0][c][kq][tid & 15] = g[c]; red[1][c][kq][tid & 15] = u[c]; }
  __syncthreads();
  if (tid < 128) { int c = tid >> 4, ff = tid & 15; float gg = 0, uu = 0; for (int q = 0; q < 16; ++q) { gg += red[0][c][q][ff]; uu += red[1][c][q][ff]; }
    gg = rbf(gg); uu = rbf(uu); float sg = rbf(gg / (1.f + __expf(-gg))); gu[c * F + blockIdx.x * 16 + ff] = f2bf(sg * uu); }
}
__global__ void k_down(const u16* gu, const u16* wd, const int* idx, const u16* rbuf, const u16* hbuf, const u16* hnbuf, u16* out) {
  __shared__ float red[C][16][16];
  int tid = threadIdx.x; int e = blockIdx.x * 16 + (tid & 15), kq = tid >> 4;
  float y[C]; for (int c = 0; c < C; ++c) y[c] = 0;
  for (int f = kq * 112; f < kq * 112 + 112; ++f) { float w = bf2f(wd[f * D + e]); for (int c = 0; c < C; ++c) y[c] += bf2f(gu[c * F + f]) * w; }
  for (int c = 0; c < C; ++c) red[c][kq][tid & 15] = y[c];
  __syncthreads();
  if (tid < 128) { int c = tid >> 4, ee = tid & 15; float s = 0; for (int q = 0; q < 16; ++q) s += red[c][q][ee];
    int t = idx[c]; float rs = rbf(1.f / (1.f + __expf(-bf2f(rbuf[t])))); float yy = rbf(rbf(s) * rs);
    int o = t * D + blockIdx.x * 16 + ee; out[o] = f2bf(bf2f(hbuf[o]) + rbf(bf2f(hnbuf[o]) + yy)); }
}
extern "C" void kernel_launch(void* const* d_in, const int* in_sizes, int n_in, void* d_out, int out_size, void* d_ws, size_t ws_size, hipStream_t stream) {
  const u16* x = (const u16*)d_in[0]; const u16* cre = (const u16*)d_in[1]; const u16* cim = (const u16*)d_in[2];
  const u16* wre = (const u16*)d_in[3]; const u16* wim = (const u16*)d_in[4];
  const u16* wr = (const u16*)d_in[6]; const u16* wg = (const u16*)d_in[7]; const u16* wu = (const u16*)d_in[8]; const u16* wd = (const u16*)d_in[9];
  char* ws = (char*)d_ws;
  u16* xn = (u16*)ws; u16* abT = (u16*)(ws + (2 << 20)); u16* Y = (u16*)(ws + (8 << 20)); u16* hb = (u16*)(ws + (14 << 20)); u16* hnb = (u16*)(ws + (16 << 20));
  u16* rb = (u16*)(ws + (18 << 20)); u16* gu = (u16*)(ws + (19 << 20)); int* idx = (int*)(ws + (20 << 20));
  u16* out = (u16*)d_out;
  k_ln<<<S / 4, 256, 0, stream>>>(x, xn);
  k_g1<<<dim3(S / 64, 896 / 16), 256, 0, stream>>>(xn, wre, wim, abT);
  k_g2<<<dim3(S / 64, 4, 2), 256, 0, stream>>>(cre, cim, abT, Y);
  k_row<<<S / 4, 256, 0, stream>>>(x, Y, wr, hb, hnb, rb, out);
  k_gu<<<F / 16, 256, 0, stream>>>(rb, hnb, wg, wu, gu, idx);
  k_down<<<D / 16, 256, 0, stream>>>(gu, wd, idx, rb, hb, hnb, out);
}
```

```cpp
#include <hip/hip_runtime.h>
#include <hip/hip_bf16.h>
typedef unsigned short u16;
typedef short bf16x8 __attribute__((ext_vector_type(8)));
typedef float f32x4 __attribute__((ext_vector_type(4)));
typedef unsigned v4u __attribute__((ext_vector_type(4)));
constexpr int S = 2048, D = 448, F = 1792, C = 8;

__device__ __forceinline__ float bf2f(u16 v) { return __uint_as_float((unsigned)v << 16); }
__device__ __forceinline__ u16 f2bf(float f) { __hip_bfloat16 h = __float2bfloat16(f); return *(u16*)&h; }
__device__ __forceinline__ float rbf(float f) { return bf2f(f2bf(f)); }
__device__ __forceinline__ float wsum(float v) { for (int o = 32; o; o >>= 1) v += __shfl_xor(v, o); return v; }

__device__ __forceinline__ void ln8(const float (&x)[8], bool act, float (&y)[8]) {
  float s = 0; for (int j = 0; j < 8; ++j) s += act ? x[j] : 0.f;
  float mean = wsum(s) * (1.f / D);
  float q = 0; for (int j = 0; j < 8; ++j) { float d = x[j] - mean; q += act ? d * d : 0.f; }
  float var = wsum(q) * (1.f / D);
  float m = rbf(mean), v = rbf(var);
  float sd = rbf(sqrtf(rbf(v + 1e-5f)));
  for (int j = 0; j < 8; ++j) y[j] = rbf(rbf(x[j] - m) / sd);
}

__global__ void k_ln(const u16* x, u16* xn) {
  int row = blockIdx.x * 4 + (threadIdx.x >> 6), lane = threadIdx.x & 63; bool act = lane < 56;
  float v[8], y[8];
  for (int j = 0; j < 8; ++j) v[j] = act ? bf2f(x[row * D + lane * 8 + j]) : 0.f;
  ln8(v, act, y);
  if (act) for (int j = 0; j < 8; ++j) xn[row * D + lane * 8 + j] = f2bf(y[j]);
}
__global__ void k_g1(const u16* xn, const u16* wre, const u16* wim, u16* abT) {
  int lane = threadIdx.x & 63, w = threadIdx.x >> 6;
  int e0 = blockIdx.y * 16, s0 = blockIdx.x * 64 + w * 16;
  const u16* W = e0 < D ? wre + e0 * D : wim + (e0 - D) * D;
  f32x4 acc = {0,0,0,0};
  for (int k = 0; k < D; k += 32) {
    bf16x8 a = *(const bf16x8*)(xn + (s0 + (lane & 15)) * D + k + (lane >> 4) * 8);
    bf16x8 b = *(const bf16x8*)(W + (lane & 15) * D + k + (lane >> 4) * 8);
    acc = __builtin_amdgcn_mfma_f32_16x16x32_bf16(a, b, acc, 0, 0, 0);
  }
  for (int j = 0; j < 4; ++j) abT[(e0 + (lane & 15)) * S + s0 + (lane >> 4) * 4 + j] = f2bf(acc[j]);
}
__global__ void k_g2(const u16* cre, const u16* cim, const u16* abT, u16* Y) {
  int lane = threadIdx.x & 63, w = threadIdx.x >> 6;
  int p = blockIdx.z, n0 = blockIdx.y * 112, t0 = blockIdx.x * 64 + w * 16;
  const u16* Cm = p ? cim : cre; const u16* B = abT + (size_t)p * D * S;
  f32x4 acc[7];
  for (int n = 0; n < 7; ++n) acc[n] = (f32x4){0,0,0,0};
  for (int k = 0; k < S; k += 32) {
    bf16x8 a = *(const bf16x8*)(Cm + (size_t)(t0 + (lane & 15)) * S + k + (lane >> 4) * 8);
    #pragma unroll
    for (int n = 0; n < 7; ++n) {
      bf16x8 b = *(const bf16x8*)(B + (size_t)(n0 + n * 16 + (lane & 15)) * S + k + (lane >> 4) * 8);
      acc[n] = __builtin_amdgcn_mfma_f32_16x16x32_bf16(b, a, acc[n], 0, 0, 0);
    }
  }
  for (int n = 0; n < 7; ++n) for (int j = 0; j < 4; ++j)
    Y[((size_t)p * S + t0 + (lane & 15)) * D + n0 + n * 16 + (lane >> 4) * 4 + j] = f2bf(acc[n][j]);
}
__global__ void k_row(const u16* x, const u16* Y, const u16* wr, u16* hbuf, u16* hnbuf, u16* rbuf, u16* out) {
  int row = blockIdx.x * 4 + (threadIdx.x >> 6), lane = threadIdx.x & 63; bool act = lane < 56;
  float h[8], hn[8];
  for (int j = 0; j < 8; ++j) {
    int e = lane * 8 + j;
    h[j] = act ? rbf(bf2f(x[row * D + e]) + rbf(bf2f(Y[row * D + e]) - bf2f(Y[(size_t)(S + row) * D + e]))) : 0.f;
  }
  ln8(h, act, hn);
  float r = 0; for (int j = 0; j < 8; ++j) r += act ? hn[j] * bf2f(wr[lane * 8 + j]) : 0.f;
  r = wsum(r);
  if (lane == 0) rbuf[row] = f2bf(r);
  if (act) for (int j = 0; j < 8; ++j) { int e = row * D + lane * 8 + j; hbuf[e] = f2bf(h[j]); hnbuf[e] = f2bf(hn[j]); out[e] = f2bf(h[j] + hn[j]); }
}
__device__ __forceinline__ unsigned okey(u16 v, int idx) { unsigned u = v; u = (u & 0x8000) ? (~u & 0xffff) : (u | 0x8000); return (u << 16) | (unsigned)(2047 - idx); }
__global__ void k_gu(const u16* rbuf, const u16* hnbuf, const u16* wg, const u16* wu, u16* gu, int* idxout) {
  __shared__ unsigned keys[S]; __shared__ int sel[C]; __shared__ float xs[C][D]; __shared__ float red[2][C][16][16];
  int tid = threadIdx.x;
  for (int i = tid; i < S; i += 256) keys[i] = okey(rbuf[i], i);
  __syncthreads();
  for (int i = tid; i < S; i += 256) { unsigned k = keys[i]; int c = 0; for (int j = 0; j < S; ++j) c += keys[j] > k; if (c < C) sel[c] = i; }
  __syncthreads();
  if (blockIdx.x == 0 && tid < C) idxout[tid] = sel[tid];
  for (int i = tid; i < C * D; i += 256) xs[i / D][i % D] = bf2f(hnbuf[sel[i / D] * D + i % D]);
  __syncthreads();
  int f = blockIdx.x * 16 + (tid & 15), kq = tid >> 4;
  float g[C], u[C]; for (int c = 0; c < C; ++c) g[c] = u[c] = 0;
  for (int d = kq * 28; d < kq * 28 + 28; ++d) { float a = bf2f(wg[d * F + f]), b = bf2f(wu[d * F + f]); for (int c = 0; c < C; ++c) { g[c] += xs[c][d] * a; u[c] += xs[c][d] * b; } }
  for (int c = 0; c < C; ++c) { red[0][c][kq][tid & 15] = g[c]; red[1][c][kq][tid & 15] = u[c]; }
  __syncthreads();
  if (tid < 128) { int c = tid >> 4, ff = tid & 15; float gg = 0, uu = 0; for (int q = 0; q < 16; ++q) { gg += red[0][c][q][ff]; uu += red[1][c][q][ff]; }
    gg = rbf(gg); uu = rbf(uu); float sg = rbf(gg / (1.f + __expf(-gg))); gu[c * F + blockIdx.x * 16 + ff] = f2bf(sg * uu); }
}
__global__ void k_down(const u16* gu, const u16* wd, const int* idx, const u16* rbuf, const u16* hbuf, const u16* hnbuf, u16* out) {
  __shared__ float red[C][16][16];
  int tid = threadIdx.x; int e = blockIdx.x * 16 + (tid & 15), kq = tid >> 4;
  float y[C]; for (int c = 0; c < C; ++c) y[c] = 0;
  for (int f = kq * 112; f < kq * 112 + 112; ++f) { float w = bf2f(wd[f * D + e]); for (int c = 0; c < C; ++c) y[c] += bf2f(gu[c * F + f]) * w; }
  for (int c = 0; c < C; ++c) red[c][kq][tid & 15] = y[c];
  __syncthreads();
  if (tid < 128) { int c = tid >> 4, ee = tid & 15; float s = 0; for (int q = 0; q < 16; ++q) s += red[c][q][ee];
    int t = idx[c]; float rs = rbf(1.f / (1.f + __expf(-bf2f(rbuf[t])))); float yy = rbf(rbf(s) * rs);
    int o = t * D + blockIdx.x * 16 + ee; out[o] = f2bf(bf2f(hbuf[o]) + rbf(bf2f(hnbuf[o]) + yy)); }
}
__global__ void k_probe(const unsigned* p, unsigned nlines, unsigned* sink, int n) {
  unsigned idx = threadIdx.x == 0 ? 1u : 0u, acc = 0;
  if (threadIdx.x == 0) {
    for (int i = 0; i < n; ++i) { unsigned v = __builtin_nontemporal_load(p + (size_t)idx * 32); acc += v; idx = (idx + 4099u + (v & 0u) + (acc == 0x9e3779b9u ? 1u : 0u)) % nlines; }
    if (acc == 0x12345678u) sink[0] = acc;
  }
}
extern "C" void kernel_launch(void* const* d_in, const int* in_sizes, int n_in, void* d_out, int out_size, void* d_ws, size_t ws_size, hipStream_t stream) {
  const u16* x = (const u16*)d_in[0]; const u16* cre = (const u16*)d_in[1]; const u16* cim = (const u16*)d_in[2];
  const u16* wre = (const u16*)d_in[3]; const u16* wim = (const u16*)d_in[4];
  const u16* wr = (const u16*)d_in[6]; const u16* wg = (const u16*)d_in[7]; const u16* wu = (const u16*)d_in[8]; const u16* wd = (const u16*)d_in[9];
  char* ws = (char*)d_ws;
  u16* xn = (u16*)ws; u16* abT = (u16*)(ws + (2 << 20)); u16* Y = (u16*)(ws + (8 << 20)); u16* hb = (u16*)(ws + (14 << 20)); u16* hnb = (u16*)(ws + (16 << 20));
  u16* rb = (u16*)(ws + (18 << 20)); u16* gu = (u16*)(ws + (19 << 20)); int* idx = (int*)(ws + (20 << 20));
  u16* out = (u16*)d_out;
  k_probe<<<1, 64, 0, stream>>>((const unsigned*)d_in[5], (unsigned)(S * D * 2 / 128), (unsigned*)(ws + (30 << 20)), 0);
  k_ln<<<S / 4, 256, 0, stream>>>(x, xn);
  k_g1<<<dim3(S / 64, 896 / 16), 256, 0, stream>>>(xn, wre, wim, abT);
  k_g2<<<dim3(S / 64, 4, 2), 256, 0, stream>>>(cre, cim, abT, Y);
  k_row<<<S / 4, 256, 0, stream>>>(x, Y, wr, hb, hnb, rb, out);
  k_gu<<<F / 16, 256, 0, stream>>>(rb, hnb, wg, wu, gu, idx);
  k_down<<<D / 16, 256, 0, stream>>>(gu, wd, idx, rb, hb, hnb, out);
}
```

```cpp
#include <hip/hip_runtime.h>
#include <hip/hip_bf16.h>

typedef unsigned short u16;
typedef unsigned v4u __attribute__((ext_vector_type(4)));
typedef unsigned v2u __attribute__((ext_vector_type(2)));
typedef float f32x4 __attribute__((ext_vector_type(4)));
typedef float f32x2 __attribute__((ext_vector_type(2)));
typedef __bf16 bf16x2 __attribute__((ext_vector_type(2)));
typedef short bf16x8 __attribute__((ext_vector_type(8)));
#define DI __device__ __forceinline__
#define GAS __attribute__((address_space(1)))
typedef GAS unsigned gu32;
typedef GAS unsigned long long gu64;
#define RLX_AGENT __ATOMIC_RELAXED, __HIP_MEMORY_SCOPE_AGENT

constexpr int S = 2048, D = 448, F = 1792;
constexpr int ROWB = D * 2;
constexpr int CROWB = S * 2;

constexpr size_t WS_EPOCH = 0;
constexpr size_t WS_FLAG1 = 4096;
constexpr size_t WS_FLAG2 = WS_FLAG1 + 32768;
constexpr size_t WS_RG = WS_FLAG2 + 32768;
constexpr size_t WS_SEL = WS_RG + 8192;
constexpr size_t WS_XS = WS_SEL + 1024;
constexpr size_t WS_HS = WS_XS + 7168;
constexpr size_t WS_GU = WS_HS + 7168;
constexpr size_t WS_ST = WS_GU + 28672;
constexpr size_t WS_BF = 1u << 20;
constexpr size_t WS_Y = 5u << 20;
constexpr int Y_BYTES = 8 * S * 448;

constexpr int P1_CHUNK = 22528;
constexpr int P1_STAT = 7 * P1_CHUNK;
constexpr int RED_RS = 912, RED_SRC = 64 * RED_RS;
constexpr int LDS_BYTES = 163840;
constexpr unsigned SPIN_CAP = 1u << 18;

#define ACLOB "a0","a1","a2","a3","a4","a5","a6","a7","a8","a9","a10","a11","a12","a13","a14","a15","a16","a17","a18","a19","a20","a21","a22","a23","a24","a25","a26","a27","a28","a29","a30","a31","a32","a33","a34","a35","a36","a37","a38","a39","a40","a41","a42","a43","a44","a45","a46","a47","a48","a49","a50","a51","a52","a53","a54","a55","a56","a57","a58","a59","a60","a61","a62","a63","a64","a65","a66","a67","a68","a69","a70","a71","a72","a73","a74","a75","a76","a77","a78","a79","a80","a81","a82","a83","a84","a85","a86","a87","a88","a89","a90","a91","a92","a93","a94","a95","a96","a97","a98","a99","a100","a101","a102","a103","a104","a105","a106","a107","a108","a109","a110","a111","a112","a113","a114","a115","a116","a117","a118","a119","a120","a121","a122","a123","a124","a125","a126","a127","a128","a129","a130","a131","a132","a133","a134","a135","a136","a137","a138","a139","a140","a141","a142","a143","a144","a145","a146","a147","a148","a149","a150","a151","a152","a153","a154","a155","a156","a157","a158","a159","a160","a161","a162","a163","a164","a165","a166","a167","a168","a169","a170","a171","a172","a173","a174","a175","a176","a177","a178","a179","a180","a181","a182","a183","a184","a185","a186","a187","a188","a189","a190","a191","a192","a193","a194","a195","a196","a197","a198","a199","a200","a201","a202","a203","a204","a205","a206","a207","a208","a209","a210","a211","a212","a213","a214","a215","a216","a217","a218","a219","a220","a221","a222","a223","a224","a225","a226","a227","a228","a229","a230","a231","a232","a233","a234","a235","a236","a237","a238","a239","a240","a241","a242","a243","a244","a245","a246","a247","a248","a249","a250","a251","a252","a253","a254","a255"

DI unsigned rfl(unsigned x) { return __builtin_amdgcn_readfirstlane(x); }
DI unsigned cvtpk(float lo, float hi) { f32x2 v = {lo, hi}; bf16x2 b = __builtin_convertvector(v, bf16x2); return __builtin_bit_cast(unsigned, b); }
DI float bflo(unsigned v) { return __uint_as_float(v << 16); }
DI float bfhi(unsigned v) { return __uint_as_float(v & 0xffff0000u); }
DI float rbf(float f) { return bflo(cvtpk(f, f)); }
DI float inv_std(float var) { return __builtin_amdgcn_rcpf(rbf(__builtin_amdgcn_sqrtf(rbf(rbf(fmaxf(var, 0.f)) + 1e-5f)))); }
template <int N> DI void wait_vm() { asm volatile("s_waitcnt vmcnt(%c0)" :: "i"(N) : "memory"); }
template <int N> DI void wait_lgkm() { asm volatile("s_waitcnt lgkmcnt(%c0)" :: "i"(N) : "memory"); }
DI void barrier() { asm volatile("s_barrier" ::: "memory"); }

constexpr int CODE_LINES = 311;
constexpr int TAIL_LINES = 160;

template <int LOFF, int SOFF, int NT> DI void dma_piece(__amdgpu_buffer_rsrc_t rs, unsigned voff, unsigned ldsbase, unsigned sbase) {
  unsigned tmp;
  if constexpr (NT)
    asm volatile("s_add_u32 m0, %2, %c4\n\ts_add_u32 %0, %3, %c5\n\tbuffer_load_dwordx4 %1, %6, %0 offen nt lds"
                 : "=&s"(tmp) : "v"(voff), "s"(ldsbase), "s"(sbase), "i"(LOFF), "i"(SOFF), "s"(rs) : "memory", "scc");
  else
    asm volatile("s_add_u32 m0, %2, %c4\n\ts_add_u32 %0, %3, %c5\n\tbuffer_load_dwordx4 %1, %6, %0 offen lds"
                 : "=&s"(tmp) : "v"(voff), "s"(ldsbase), "s"(sbase), "i"(LOFF), "i"(SOFF), "s"(rs) : "memory", "scc");
}
DI void touch(unsigned& sink, const void* lane_addr) { asm volatile("global_load_dword %0, %1, off" : "=v"(sink) : "v"(lane_addr) : "memory"); }
template <int OFF> DI void lds_read128(bf16x8& dst, unsigned addr) {
  asm volatile("ds_read_b128 %0, %1 offset:%c2" : "=v"(dst) : "v"(addr), "i"(OFF) : "memory");
}
DI void mfma_new(f32x4& acc, const bf16x8& a, const bf16x8& b) {
  asm volatile("v_mfma_f32_16x16x32_bf16 %0, %1, %2, 0" : "=&v"(acc) : "v"(a), "v"(b));
}
DI void mfma_acc(f32x4& acc, const bf16x8& a, const bf16x8& b) {
  asm volatile("v_mfma_f32_16x16x32_bf16 %0, %1, %2, %0" : "+v"(acc) : "v"(a), "v"(b));
}

struct Args { const u16 *x, *cre, *cim, *wre, *wim, *wr, *wg, *wu, *wd; u16* out; char* ws; };
struct P1Ctx { __amdgpu_buffer_rsrc_t rsX, rsW; unsigned vx, vw, sx, sw, lds0; };
struct ACtx { __amdgpu_buffer_rsrc_t rsC; unsigned v, ldsw, src[8], r0, r1, r0h, r1h; };
struct P1Acc { f32x4 acc[2][4]; f32x4 aux[3]; };

DI void st16_sc1(void* base, unsigned off, const v4u& v) { asm volatile("global_store_dwordx4 %0, %1, %2 sc1\n\ts_nop 1" :: "v"(off), "v"(v), "s"(base) : "memory"); }
template <int AR, int OFF> DI void a_read(unsigned addr) {
  asm volatile("ds_read_b128 a[%c1:%c2], %0 offset:%c3" :: "v"(addr), "i"(AR), "i"(AR + 3), "i"(OFF) : "memory", ACLOB);
}
template <int IMM> DI void b_load(bf16x8& dst, unsigned vlane, const char* g) {
  asm volatile("global_load_dwordx4 %0, %1, %2 offset:%c3 sc1" : "=v"(dst) : "v"(vlane), "s"(g), "i"(IMM) : "memory");
}
template <int FIRST, int CNT, int A0> DI void grp(f32x4& c0, f32x4& c1, f32x4& c2, f32x4& c3, const bf16x8& b) {
  if constexpr (FIRST)
    asm volatile("s_waitcnt vmcnt(%c5)\n\t"
                 "v_mfma_f32_16x16x32_bf16 %0, %4, a[%c6:%c7], 0\n\tv_mfma_f32_16x16x32_bf16 %1, %4, a[%c8:%c9], 0\n\t"
                 "v_mfma_f32_16x16x32_bf16 %2, %4, a[%c10:%c11], 0\n\tv_mfma_f32_16x16x32_bf16 %3, %4, a[%c12:%c13], 0"
                 : "=&v"(c0), "=&v"(c1), "=&v"(c2), "=&v"(c3)
                 : "v"(b), "i"(CNT), "i"(A0), "i"(A0 + 3), "i"(A0 + 4), "i"(A0 + 7), "i"(A0 + 8), "i"(A0 + 11), "i"(A0 + 12), "i"(A0 + 15) : "memory");
  else
    asm volatile("s_waitcnt vmcnt(%c5)\n\t"
                 "v_mfma_f32_16x16x32_bf16 %0, %4, a[%c6:%c7], %0\n\tv_mfma_f32_16x16x32_bf16 %1, %4, a[%c8:%c9], %1\n\t"
                 "v_mfma_f32_16x16x32_bf16 %2, %4, a[%c10:%c11], %2\n\tv_mfma_f32_16x16x32_bf16 %3, %4, a[%c12:%c13], %3"
                 : "+v"(c0), "+v"(c1), "+v"(c2), "+v"(c3)
                 : "v"(b), "i"(CNT), "i"(A0), "i"(A0 + 3), "i"(A0 + 4), "i"(A0 + 7), "i"(A0 + 8), "i"(A0 + 11), "i"(A0 + 12), "i"(A0 + 15) : "memory");
}
template <int OFF> DI void lds_write128(unsigned addr, const f32x4& v) {
  asm volatile("ds_write_b128 %0, %1 offset:%c2" :: "v"(addr), "v"(v), "i"(OFF) : "memory");
}

#define GEN_P1_W0 do { \
  dma_piece<0, 0, 0>(c.rsX, c.vx, c.lds0, c.sx); \
  dma_piece<4096, 28672, 0>(c.rsX, c.vx, c.lds0, c.sx); \
  dma_piece<8192, 0, 0>(c.rsW, c.vw, c.lds0, c.sw); \
  dma_piece<12288, 28672, 0>(c.rsW, c.vw, c.lds0, c.sw); \
  dma_piece<16384, 57344, 0>(c.rsW, c.vw, c.lds0, c.sw); \
  dma_piece<20480, 86016, 0>(c.rsW, c.vw, c.lds0, c.sw); \
  dma_piece<22528, 128, 0>(c.rsX, c.vx, c.lds0, c.sx); \
  dma_piece<26624, 28800, 0>(c.rsX, c.vx, c.lds0, c.sx); \
  dma_piece<30720, 128, 0>(c.rsW, c.vw, c.lds0, c.sw); \
  dma_piece<34816, 28800, 0>(c.rsW, c.vw, c.lds0, c.sw); \
  dma_piece<38912, 57472, 0>(c.rsW, c.vw, c.lds0, c.sw); \
  dma_piece<43008, 86144, 0>(c.rsW, c.vw, c.lds0, c.sw); \
  XW_TOUCH(); \
  dma_piece<45056, 256, 0>(c.rsX, c.vx, c.lds0, c.sx); \
  dma_piece<49152, 28928, 0>(c.rsX, c.vx, c.lds0, c.sx); \
  dma_piece<53248, 256, 0>(c.rsW, c.vw, c.lds0, c.sw); \
  dma_piece<57344, 28928, 0>(c.rsW, c.vw, c.lds0, c.sw); \
  dma_piece<61440, 57600, 0>(c.rsW, c.vw, c.lds0, c.sw); \
  dma_piece<65536, 86272, 0>(c.rsW, c.vw, c.lds0, c.sw); \
  A_SETUP(); \
  P1_INIT(); \
  wait_vm<13>(); \
  barrier(); \
  lds_read128<0>(xa0[0], f0); \
  lds_read128<8192>(wb0[0], f0); \
  lds_read128<2048>(xa0[1], f0); \
  lds_read128<10240>(wb0[1], f0); \
  lds_read128<12288>(wb0[2], f0); \
  lds_read128<14336>(wb0[3], f0); \
  lds_read128<0>(xa1[0], f1); \
  lds_read128<8192>(wb1[0], f1); \
  lds_read128<2048>(xa1[1], f1); \
  lds_read128<10240>(wb1[1], f1); \
  lds_read128<12288>(wb1[2], f1); \
  lds_read128<14336>(wb1[3], f1); \
  asm volatile("s_waitcnt lgkmcnt(6)" : "+v"(xa0[0]), "+v"(xa0[1]), "+v"(wb0[0]), "+v"(wb0[1]), "+v"(wb0[2]), "+v"(wb0[3]) :: "memory"); \
  mfma_acc(a.acc[0][0], xa0[0], wb0[0]); \
  mfma_acc(a.acc[1][0], xa0[1], wb0[0]); \
  mfma_acc(a.acc[0][1], xa0[0], wb0[1]); \
  dma_piece<67584, 384, 0>(c.rsX, c.vx, c.lds0, c.sx); \
  mfma_acc(a.acc[1][1], xa0[1], wb0[1]); \
  mfma_acc(a.acc[0][2], xa0[0], wb0[2]); \
  mfma_acc(a.acc[1][2], xa0[1], wb0[2]); \
  dma_piece<71680, 29056, 0>(c.rsX, c.vx, c.lds0, c.sx); \
  mfma_acc(a.acc[0][3], xa0[0], wb0[3]); \
  mfma_acc(a.acc[1][3], xa0[1], wb0[3]); \
  mfma_acc(a.aux[0], ones, wb0[0]); \
  dma_piece<75776, 384, 0>(c.rsW, c.vw, c.lds0, c.sw); \
  mfma_acc(a.aux[1], ones, wb0[1]); \
  mfma_acc(a.aux[2], ones, wb0[2]); \
  asm volatile("s_waitcnt lgkmcnt(0)" : "+v"(xa1[0]), "+v"(xa1[1]), "+v"(wb1[0]), "+v"(wb1[1]), "+v"(wb1[2]), "+v"(wb1[3]) :: "memory"); \
  wait_vm<10>(); \
  barrier(); \
  f0 += 22528; f1 += 22528; \
  lds_read128<0>(xa0[0], f0); \
  lds_read128<8192>(wb0[0], f0); \
  lds_read128<2048>(xa0[1], f0); \
  lds_read128<10240>(wb0[1], f0); \
  lds_read128<12288>(wb0[2], f0); \
  lds_read128<14336>(wb0[3], f0); \
  mfma_acc(a.acc[0][0], xa1[0], wb1[0]); \
  mfma_acc(a.acc[1][0], xa1[1], wb1[0]); \
  mfma_acc(a.acc[0][1], xa1[0], wb1[1]); \
  dma_piece<79872, 29056, 0>(c.rsW, c.vw, c.lds0, c.sw); \
  mfma_acc(a.acc[1][1], xa1[1], wb1[1]); \
  mfma_acc(a.acc[0][2], xa1[0], wb1[2]); \
  mfma_acc(a.acc[1][2], xa1[1], wb1[2]); \
  dma_piece<83968, 57728, 0>(c.rsW, c.vw, c.lds0, c.sw); \
  mfma_acc(a.acc[0][3], xa1[0], wb1[3]); \
  mfma_acc(a.acc[1][3], xa1[1], wb1[3]); \
  mfma_acc(a.aux[0], ones, wb1[0]); \
  dma_piece<88064, 86400, 0>(c.rsW, c.vw, c.lds0, c.sw); \
  mfma_acc(a.aux[1], ones, wb1[1]); \
  mfma_acc(a.aux[2], ones, wb1[2]); \
  lds_read128<0>(xa1[0], f1); \
  lds_read128<8192>(wb1[0], f1); \
  lds_read128<2048>(xa1[1], f1); \
  lds_read128<10240>(wb1[1], f1); \
  lds_read128<12288>(wb1[2], f1); \
  lds_read128<14336>(wb1[3], f1); \
  asm volatile("s_waitcnt lgkmcnt(6)" : "+v"(xa0[0]), "+v"(xa0[1]), "+v"(wb0[0]), "+v"(wb0[1]), "+v"(wb0[2]), "+v"(wb0[3]) :: "memory"); \
  mfma_acc(a.acc[0][0], xa0[0], wb0[0]); \
  mfma_acc(a.acc[1][0], xa0[1], wb0[0]); \
  mfma_acc(a.acc[0][1], xa0[0], wb0[1]); \
  dma_piece<90112, 512, 0>(c.rsX, c.vx, c.lds0, c.sx); \
  mfma_acc(a.acc[1][1], xa0[1], wb0[1]); \
  mfma_acc(a.acc[0][2], xa0[0], wb0[2]); \
  mfma_acc(a.acc[1][2], xa0[1], wb0[2]); \
  dma_piece<94208, 29184, 0>(c.rsX, c.vx, c.lds0, c.sx); \
  mfma_acc(a.acc[0][3], xa0[0], wb0[3]); \
  mfma_acc(a.acc[1][3], xa0[1], wb0[3]); \
  mfma_acc(a.aux[0], ones, wb0[0]); \
  dma_piece<98304, 512, 0>(c.rsW, c.vw, c.lds0, c.sw); \
  mfma_acc(a.aux[1], ones, wb0[1]); \
  mfma_acc(a.aux[2], ones, wb0[2]); \
  asm volatile("s_waitcnt lgkmcnt(0)" : "+v"(xa1[0]), "+v"(xa1[1]), "+v"(wb1[0]), "+v"(wb1[1]), "+v"(wb1[2]), "+v"(wb1[3]) :: "memory"); \
  wait_vm<9>(); \
  barrier(); \
  f0 += 22528; f1 += 22528; \
  lds_read128<0>(xa0[0], f0); \
  lds_read128<8192>(wb0[0], f0); \
  lds_read128<2048>(xa0[1], f0); \
  lds_read128<10240>(wb0[1], f0); \
  lds_read128<12288>(wb0[2], f0); \
  lds_read128<14336>(wb0[3], f0); \
  mfma_acc(a.acc[0][0], xa1[0], wb1[0]); \
  mfma_acc(a.acc[1][0], xa1[1], wb1[0]); \
  mfma_acc(a.acc[0][1], xa1[0], wb1[1]); \
  dma_piece<102400, 29184, 0>(c.rsW, c.vw, c.lds0, c.sw); \
  mfma_acc(a.acc[1][1], xa1[1], wb1[1]); \
  mfma_acc(a.acc[0][2], xa1[0], wb1[2]); \
  mfma_acc(a.acc[1][2], xa1[1], wb1[2]); \
  dma_piece<106496, 57856, 0>(c.rsW, c.vw, c.lds0, c.sw); \
  mfma_acc(a.acc[0][3], xa1[0], wb1[3]); \
  mfma_acc(a.acc[1][3], xa1[1], wb1[3]); \
  mfma_acc(a.aux[0], ones, wb1[0]); \
  dma_piece<110592, 86528, 0>(c.rsW, c.vw, c.lds0, c.sw); \
  mfma_acc(a.aux[1], ones, wb1[1]); \
  mfma_acc(a.aux[2], ones, wb1[2]); \
  lds_read128<0>(xa1[0], f1); \
  lds_read128<8192>(wb1[0], f1); \
  lds_read128<2048>(xa1[1], f1); \
  lds_read128<10240>(wb1[1], f1); \
  lds_read128<12288>(wb1[2], f1); \
  lds_read128<14336>(wb1[3], f1); \
  asm volatile("s_waitcnt lgkmcnt(6)" : "+v"(xa0[0]), "+v"(xa0[1]), "+v"(wb0[0]), "+v"(wb0[1]), "+v"(wb0[2]), "+v"(wb0[3]) :: "memory"); \
  mfma_acc(a.acc[0][0], xa0[0], wb0[0]); \
  mfma_acc(a.acc[1][0], xa0[1], wb0[0]); \
  mfma_acc(a.acc[0][1], xa0[0], wb0[1]); \
  dma_piece<112640, 640, 0>(c.rsX, c.vx, c.lds0, c.sx); \
  mfma_acc(a.acc[1][1], xa0[1], wb0[1]); \
  mfma_acc(a.acc[0][2], xa0[0], wb0[2]); \
  mfma_acc(a.acc[1][2], xa0[1], wb0[2]); \
  dma_piece<116736, 29312, 0>(c.rsX, c.vx, c.lds0, c.sx); \
  mfma_acc(a.acc[0][3], xa0[0], wb0[3]); \
  mfma_acc(a.acc[1][3], xa0[1], wb0[3]); \
  mfma_acc(a.aux[0], ones, wb0[0]); \
  dma_piece<120832, 640, 0>(c.rsW, c.vw, c.lds0, c.sw); \
  mfma_acc(a.aux[1], ones, wb0[1]); \
  mfma_acc(a.aux[2], ones, wb0[2]); \
  asm volatile("s_waitcnt lgkmcnt(0)" : "+v"(xa1[0]), "+v"(xa1[1]), "+v"(wb1[0]), "+v"(wb1[1]), "+v"(wb1[2]), "+v"(wb1[3]) :: "memory"); \
  wait_vm<9>(); \
  barrier(); \
  f0 += 22528; f1 += 22528; \
  lds_read128<0>(xa0[0], f0); \
  lds_read128<8192>(wb0[0], f0); \
  lds_read128<2048>(xa0[1], f0); \
  lds_read128<10240>(wb0[1], f0); \
  lds_read128<12288>(wb0[2], f0); \
  lds_read128<14336>(wb0[3], f0); \
  mfma_acc(a.acc[0][0], xa1[0], wb1[0]); \
  mfma_acc(a.acc[1][0], xa1[1], wb1[0]); \
  mfma_acc(a.acc[0][1], xa1[0], wb1[1]); \
  dma_piece<124928, 29312, 0>(c.rsW, c.vw, c.lds0, c.sw); \
  mfma_acc(a.acc[1][1], xa1[1], wb1[1]); \
  mfma_acc(a.acc[0][2], xa1[0], wb1[2]); \
  mfma_acc(a.acc[1][2], xa1[1], wb1[2]); \
  dma_piece<129024, 57984, 0>(c.rsW, c.vw, c.lds0, c.sw); \
  mfma_acc(a.acc[0][3], xa1[0], wb1[3]); \
  mfma_acc(a.acc[1][3], xa1[1], wb1[3]); \
  mfma_acc(a.aux[0], ones, wb1[0]); \
  dma_piece<133120, 86656, 0>(c.rsW, c.vw, c.lds0, c.sw); \
  mfma_acc(a.aux[1], ones, wb1[1]); \
  mfma_acc(a.aux[2], ones, wb1[2]); \
  lds_read128<0>(xa1[0], f1); \
  lds_read128<8192>(wb1[0], f1); \
  lds_read128<2048>(xa1[1], f1); \
  lds_read128<10240>(wb1[1], f1); \
  lds_read128<12288>(wb1[2], f1); \
  lds_read128<14336>(wb1[3], f1); \
  asm volatile("s_waitcnt lgkmcnt(6)" : "+v"(xa0[0]), "+v"(xa0[1]), "+v"(wb0[0]), "+v"(wb0[1]), "+v"(wb0[2]), "+v"(wb0[3]) :: "memory"); \
  mfma_acc(a.acc[0][0], xa0[0], wb0[0]); \
  mfma_acc(a.acc[1][0], xa0[1], wb0[0]); \
  mfma_acc(a.acc[0][1], xa0[0], wb0[1]); \
  dma_piece<135168, 768, 0>(c.rsX, c.vx, c.lds0, c.sx); \
  mfma_acc(a.acc[1][1], xa0[1], wb0[1]); \
  mfma_acc(a.acc[0][2], xa0[0], wb0[2]); \
  mfma_acc(a.acc[1][2], xa0[1], wb0[2]); \
  dma_piece<139264, 29440, 0>(c.rsX, c.vx, c.lds0, c.sx); \
  mfma_acc(a.acc[0][3], xa0[0], wb0[3]); \
  mfma_acc(a.acc[1][3], xa0[1], wb0[3]); \
  mfma_acc(a.aux[0], ones, wb0[0]); \
  dma_piece<143360, 768, 0>(c.rsW, c.vw, c.lds0, c.sw); \
  mfma_acc(a.aux[1], ones, wb0[1]); \
  mfma_acc(a.aux[2], ones, wb0[2]); \
  asm volatile("s_waitcnt lgkmcnt(0)" : "+v"(xa1[0]), "+v"(xa1[1]), "+v"(wb1[0]), "+v"(wb1[1]), "+v"(wb1[2]), "+v"(wb1[3]) :: "memory"); \
  wait_vm<9>(); \
  barrier(); \
  f0 += 22528; f1 += 22528; \
  lds_read128<0>(xa0[0], f0); \
  lds_read128<8192>(wb0[0], f0); \
  lds_read128<2048>(xa0[1], f0); \
  lds_read128<10240>(wb0[1], f0); \
  lds_read128<12288>(wb0[2], f0); \
  lds_read128<14336>(wb0[3], f0); \
  mfma_acc(a.acc[0][0], xa1[0], wb1[0]); \
  mfma_acc(a.acc[1][0], xa1[1], wb1[0]); \
  mfma_acc(a.acc[0][1], xa1[0], wb1[1]); \
  dma_piece<147456, 29440, 0>(c.rsW, c.vw, c.lds0, c.sw); \
  mfma_acc(a.acc[1][1], xa1[1], wb1[1]); \
  mfma_acc(a.acc[0][2], xa1[0], wb1[2]); \
  mfma_acc(a.acc[1][2], xa1[1], wb1[2]); \
  dma_piece<151552, 58112, 0>(c.rsW, c.vw, c.lds0, c.sw); \
  mfma_acc(a.acc[0][3], xa1[0], wb1[3]); \
  mfma_acc(a.acc[1][3], xa1[1], wb1[3]); \
  mfma_acc(a.aux[0], ones, wb1[0]); \
  dma_piece<155648, 86784, 0>(c.rsW, c.vw, c.lds0, c.sw); \
  mfma_acc(a.aux[1], ones, wb1[1]); \
  mfma_acc(a.aux[2], ones, wb1[2]); \
  lds_read128<0>(xa1[0], f1); \
  lds_read128<8192>(wb1[0], f1); \
  lds_read128<2048>(xa1[1], f1); \
  lds_read128<10240>(wb1[1], f1); \
  lds_read128<12288>(wb1[2], f1); \
  lds_read128<14336>(wb1[3], f1); \
  asm volatile("s_waitcnt lgkmcnt(6)" : "+v"(xa0[0]), "+v"(xa0[1]), "+v"(wb0[0]), "+v"(wb0[1]), "+v"(wb0[2]), "+v"(wb0[3]) :: "memory"); \
  mfma_acc(a.acc[0][0], xa0[0], wb0[0]); \
  mfma_acc(a.acc[1][0], xa0[1], wb0[0]); \
  mfma_acc(a.acc[0][1], xa0[0], wb0[1]); \
  dma_piece<0, 0, 1>(A.rsC, A.v, A.ldsw, A.src[0]); \
  mfma_acc(a.acc[1][1], xa0[1], wb0[1]); \
  mfma_acc(a.acc[0][2], xa0[0], wb0[2]); \
  mfma_acc(a.acc[1][2], xa0[1], wb0[2]); \
  dma_piece<2048, 65536, 1>(A.rsC, A.v, A.ldsw, A.src[0]); \
  mfma_acc(a.acc[0][3], xa0[0], wb0[3]); \
  mfma_acc(a.acc[1][3], xa0[1], wb0[3]); \
  mfma_acc(a.aux[0], ones, wb0[0]); \
  dma_piece<4096, 131072, 1>(A.rsC, A.v, A.ldsw, A.src[0]); \
  mfma_acc(a.aux[1], ones, wb0[1]); \
  mfma_acc(a.aux[2], ones, wb0[2]); \
  asm volatile("s_waitcnt lgkmcnt(0)" : "+v"(xa1[0]), "+v"(xa1[1]), "+v"(wb1[0]), "+v"(wb1[1]), "+v"(wb1[2]), "+v"(wb1[3]) :: "memory"); \
  wait_vm<9>(); \
  barrier(); \
  f0 += 22528; f1 += 22528; \
  lds_read128<0>(xa0[0], f0); \
  lds_read128<8192>(wb0[0], f0); \
  lds_read128<2048>(xa0[1], f0); \
  lds_read128<10240>(wb0[1], f0); \
  lds_read128<12288>(wb0[2], f0); \
  lds_read128<14336>(wb0[3], f0); \
  mfma_acc(a.acc[0][0], xa1[0], wb1[0]); \
  mfma_acc(a.acc[1][0], xa1[1], wb1[0]); \
  mfma_acc(a.acc[0][1], xa1[0], wb1[1]); \
  dma_piece<6144, 196608, 1>(A.rsC, A.v, A.ldsw, A.src[0]); \
  mfma_acc(a.acc[1][1], xa1[1], wb1[1]); \
  mfma_acc(a.acc[0][2], xa1[0], wb1[2]); \
  mfma_acc(a.acc[1][2], xa1[1], wb1[2]); \
  dma_piece<16384, 0, 1>(A.rsC, A.v, A.ldsw, A.src[1]); \
  mfma_acc(a.acc[0][3], xa1[0], wb1[3]); \
  mfma_acc(a.acc[1][3], xa1[1], wb1[3]); \
  mfma_acc(a.aux[0], ones, wb1[0]); \
  dma_piece<18432, 65536, 1>(A.rsC, A.v, A.ldsw, A.src[1]); \
  mfma_acc(a.aux[1], ones, wb1[1]); \
  mfma_acc(a.aux[2], ones, wb1[2]); \
  lds_read128<0>(xa1[0], f1); \
  lds_read128<8192>(wb1[0], f1); \
  lds_read128<2048>(xa1[1], f1); \
  lds_read128<10240>(wb1[1], f1); \
  lds_read128<12288>(wb1[2], f1); \
  lds_read128<14336>(wb1[3], f1); \
  asm volatile("s_waitcnt lgkmcnt(6)" : "+v"(xa0[0]), "+v"(xa0[1]), "+v"(wb0[0]), "+v"(wb0[1]), "+v"(wb0[2]), "+v"(wb0[3]) :: "memory"); \
  mfma_acc(a.acc[0][0], xa0[0], wb0[0]); \
  mfma_acc(a.acc[1][0], xa0[1], wb0[0]); \
  mfma_acc(a.acc[0][1], xa0[0], wb0[1]); \
  dma_piece<20480, 131072, 1>(A.rsC, A.v, A.ldsw, A.src[1]); \
  mfma_acc(a.acc[1][1], xa0[1], wb0[1]); \
  mfma_acc(a.acc[0][2], xa0[0], wb0[2]); \
  mfma_acc(a.acc[1][2], xa0[1], wb0[2]); \
  dma_piece<22528, 196608, 1>(A.rsC, A.v, A.ldsw, A.src[1]); \
  mfma_acc(a.acc[0][3], xa0[0], wb0[3]); \
  mfma_acc(a.acc[1][3], xa0[1], wb0[3]); \
  mfma_acc(a.aux[0], ones, wb0[0]); \
  dma_piece<32768, 0, 1>(A.rsC, A.v, A.ldsw, A.src[2]); \
  mfma_acc(a.aux[1], ones, wb0[1]); \
  mfma_acc(a.aux[2], ones, wb0[2]); \
  asm volatile("s_waitcnt lgkmcnt(0)" : "+v"(xa1[0]), "+v"(xa1[1]), "+v"(wb1[0]), "+v"(wb1[1]), "+v"(wb1[2]), "+v"(wb1[3]) :: "memory"); \
  wait_vm<9>(); \
  barrier(); \
  f0 += 22528; f1 += 22528; \
  lds_read128<0>(xa0[0], f0); \
  lds_read128<8192>(wb0[0], f0); \
  lds_read128<2048>(xa0[1], f0); \
  lds_read128<10240>(wb0[1], f0); \
  lds_read128<12288>(wb0[2], f0); \
  lds_read128<14336>(wb0[3], f0); \
  mfma_acc(a.acc[0][0], xa1[0], wb1[0]); \
  mfma_acc(a.acc[1][0], xa1[1], wb1[0]); \
  mfma_acc(a.acc[0][1], xa1[0], wb1[1]); \
  dma_piece<34816, 65536, 1>(A.rsC, A.v, A.ldsw, A.src[2]); \
  mfma_acc(a.acc[1][1], xa1[1], wb1[1]); \
  mfma_acc(a.acc[0][2], xa1[0], wb1[2]); \
  mfma_acc(a.acc[1][2], xa1[1], wb1[2]); \
  dma_piece<36864, 131072, 1>(A.rsC, A.v, A.ldsw, A.src[2]); \
  mfma_acc(a.acc[0][3], xa1[0], wb1[3]); \
  mfma_acc(a.acc[1][3], xa1[1], wb1[3]); \
  mfma_acc(a.aux[0], ones, wb1[0]); \
  dma_piece<38912, 196608, 1>(A.rsC, A.v, A.ldsw, A.src[2]); \
  mfma_acc(a.aux[1], ones, wb1[1]); \
  mfma_acc(a.aux[2], ones, wb1[2]); \
  lds_read128<0>(xa1[0], f1); \
  lds_read128<8192>(wb1[0], f1); \
  lds_read128<2048>(xa1[1], f1); \
  lds_read128<10240>(wb1[1], f1); \
  lds_read128<12288>(wb1[2], f1); \
  lds_read128<14336>(wb1[3], f1); \
  asm volatile("s_waitcnt lgkmcnt(6)" : "+v"(xa0[0]), "+v"(xa0[1]), "+v"(wb0[0]), "+v"(wb0[1]), "+v"(wb0[2]), "+v"(wb0[3]) :: "memory"); \
  mfma_acc(a.acc[0][0], xa0[0], wb0[0]); \
  mfma_acc(a.acc[1][0], xa0[1], wb0[0]); \
  mfma_acc(a.acc[0][1], xa0[0], wb0[1]); \
  dma_piece<49152, 0, 1>(A.rsC, A.v, A.ldsw, A.src[3]); \
  mfma_acc(a.acc[1][1], xa0[1], wb0[1]); \
  mfma_acc(a.acc[0][2], xa0[0], wb0[2]); \
  mfma_acc(a.acc[1][2], xa0[1], wb0[2]); \
  dma_piece<51200, 65536, 1>(A.rsC, A.v, A.ldsw, A.src[3]); \
  mfma_acc(a.acc[0][3], xa0[0], wb0[3]); \
  mfma_acc(a.acc[1][3], xa0[1], wb0[3]); \
  mfma_acc(a.aux[0], ones, wb0[0]); \
  dma_piece<53248, 131072, 1>(A.rsC, A.v, A.ldsw, A.src[3]); \
  mfma_acc(a.aux[1], ones, wb0[1]); \
  mfma_acc(a.aux[2], ones, wb0[2]); \
  asm volatile("s_waitcnt lgkmcnt(0)" : "+v"(xa1[0]), "+v"(xa1[1]), "+v"(wb1[0]), "+v"(wb1[1]), "+v"(wb1[2]), "+v"(wb1[3]) :: "memory"); \
  mfma_acc(a.acc[0][0], xa1[0], wb1[0]); \
  mfma_acc(a.acc[1][0], xa1[1], wb1[0]); \
  mfma_acc(a.acc[0][1], xa1[0], wb1[1]); \
  dma_piece<55296, 196608, 1>(A.rsC, A.v, A.ldsw, A.src[3]); \
  mfma_acc(a.acc[1][1], xa1[1], wb1[1]); \
  mfma_acc(a.acc[0][2], xa1[0], wb1[2]); \
  mfma_acc(a.acc[1][2], xa1[1], wb1[2]); \
  mfma_acc(a.acc[0][3], xa1[0], wb1[3]); \
  mfma_acc(a.acc[1][3], xa1[1], wb1[3]); \
  mfma_acc(a.aux[0], ones, wb1[0]); \
  mfma_acc(a.aux[1], ones, wb1[1]); \
  mfma_acc(a.aux[2], ones, wb1[2]); \
  P1_FINISH_STATS(); \
  P1_EPILOGUE_COMPUTE(); \
  P1_STORE(0); \
  P1_STORE(1); \
  P1_STORE(2); \
  P1_STORE(3); \
  dma_piece<65536, 0, 1>(A.rsC, A.v, A.ldsw, A.src[4]); \
  dma_piece<67584, 65536, 1>(A.rsC, A.v, A.ldsw, A.src[4]); \
  dma_piece<69632, 131072, 1>(A.rsC, A.v, A.ldsw, A.src[4]); \
  dma_piece<71680, 196608, 1>(A.rsC, A.v, A.ldsw, A.src[4]); \
  wait_vm<4>(); \
  barrier(); \
  P1_FLAG(); \
} while (0)
#define GEN_P1_W1 do { \
  dma_piece<1024, 0, 0>(c.rsX, c.vx, c.lds0, c.sx); \
  dma_piece<5120, 28672, 0>(c.rsX, c.vx, c.lds0, c.sx); \
  dma_piece<9216, 7168, 0>(c.rsW, c.vw, c.lds0, c.sw); \
  dma_piece<13312, 35840, 0>(c.rsW, c.vw, c.lds0, c.sw); \
  dma_piece<17408, 64512, 0>(c.rsW, c.vw, c.lds0, c.sw); \
  dma_piece<21504, 93184, 0>(c.rsW, c.vw, c.lds0, c.sw); \
  dma_piece<23552, 128, 0>(c.rsX, c.vx, c.lds0, c.sx); \
  dma_piece<27648, 28800, 0>(c.rsX, c.vx, c.lds0, c.sx); \
  dma_piece<31744, 7296, 0>(c.rsW, c.vw, c.lds0, c.sw); \
  dma_piece<35840, 35968, 0>(c.rsW, c.vw, c.lds0, c.sw); \
  dma_piece<39936, 64640, 0>(c.rsW, c.vw, c.lds0, c.sw); \
  dma_piece<44032, 93312, 0>(c.rsW, c.vw, c.lds0, c.sw); \
  XW_TOUCH(); \
  dma_piece<46080, 256, 0>(c.rsX, c.vx, c.lds0, c.sx); \
  dma_piece<50176, 28928, 0>(c.rsX, c.vx, c.lds0, c.sx); \
  dma_piece<54272, 7424, 0>(c.rsW, c.vw, c.lds0, c.sw); \
  dma_piece<58368, 36096, 0>(c.rsW, c.vw, c.lds0, c.sw); \
  dma_piece<62464, 64768, 0>(c.rsW, c.vw, c.lds0, c.sw); \
  dma_piece<66560, 93440, 0>(c.rsW, c.vw, c.lds0, c.sw); \
  A_SETUP(); \
  P1_INIT(); \
  wait_vm<13>(); \
  barrier(); \
  lds_read128<4096>(xa0[0], f0); \
  lds_read128<8192>(wb0[0], f0); \
  lds_read128<6144>(xa0[1], f0); \
  lds_read128<10240>(wb0[1], f0); \
  lds_read128<12288>(wb0[2], f0); \
  lds_read128<14336>(wb0[3], f0); \
  lds_read128<4096>(xa1[0], f1); \
  lds_read128<8192>(wb1[0], f1); \
  lds_read128<6144>(xa1[1], f1); \
  lds_read128<10240>(wb1[1], f1); \
  lds_read128<12288>(wb1[2], f1); \
  lds_read128<14336>(wb1[3], f1); \
  asm volatile("s_waitcnt lgkmcnt(6)" : "+v"(xa0[0]), "+v"(xa0[1]), "+v"(wb0[0]), "+v"(wb0[1]), "+v"(wb0[2]), "+v"(wb0[3]) :: "memory"); \
  mfma_acc(a.acc[0][0], xa0[0], wb0[0]); \
  mfma_acc(a.acc[1][0], xa0[1], wb0[0]); \
  mfma_acc(a.acc[0][1], xa0[0], wb0[1]); \
  dma_piece<68608, 384, 0>(c.rsX, c.vx, c.lds0, c.sx); \
  mfma_acc(a.acc[1][1], xa0[1], wb0[1]); \
  mfma_acc(a.acc[0][2], xa0[0], wb0[2]); \
  mfma_acc(a.acc[1][2], xa0[1], wb0[2]); \
  dma_piece<72704, 29056, 0>(c.rsX, c.vx, c.lds0, c.sx); \
  mfma_acc(a.acc[0][3], xa0[0], wb0[3]); \
  mfma_acc(a.acc[1][3], xa0[1], wb0[3]); \
  mfma_acc(a.aux[0], ones, wb0[3]); \
  dma_piece<76800, 7552, 0>(c.rsW, c.vw, c.lds0, c.sw); \
  mfma_acc(a.aux[1], xa0[0], xa0[0]); \
  mfma_acc(a.aux[2], xa0[1], xa0[1]); \
  asm volatile("s_waitcnt lgkmcnt(0)" : "+v"(xa1[0]), "+v"(xa1[1]), "+v"(wb1[0]), "+v"(wb1[1]), "+v"(wb1[2]), "+v"(wb1[3]) :: "memory"); \
  wait_vm<10>(); \
  barrier(); \
  f0 += 22528; f1 += 22528; \
  lds_read128<4096>(xa0[0], f0); \
  lds_read128<8192>(wb0[0], f0); \
  lds_read128<6144>(xa0[1], f0); \
  lds_read128<10240>(wb0[1], f0); \
  lds_read128<12288>(wb0[2], f0); \
  lds_read128<14336>(wb0[3], f0); \
  mfma_acc(a.acc[0][0], xa1[0], wb1[0]); \
  mfma_acc(a.acc[1][0], xa1[1], wb1[0]); \
  mfma_acc(a.acc[0][1], xa1[0], wb1[1]); \
  dma_piece<80896, 36224, 0>(c.rsW, c.vw, c.lds0, c.sw); \
  mfma_acc(a.acc[1][1], xa1[1], wb1[1]); \
  mfma_acc(a.acc[0][2], xa1[0], wb1[2]); \
  mfma_acc(a.acc[1][2], xa1[1], wb1[2]); \
  dma_piece<84992, 64896, 0>(c.rsW, c.vw, c.lds0, c.sw); \
  mfma_acc(a.acc[0][3], xa1[0], wb1[3]); \
  mfma_acc(a.acc[1][3], xa1[1], wb1[3]); \
  mfma_acc(a.aux[0], ones, wb1[3]); \
  dma_piece<89088, 93568, 0>(c.rsW, c.vw, c.lds0, c.sw); \
  mfma_acc(a.aux[1], xa1[0], xa1[0]); \
  mfma_acc(a.aux[2], xa1[1], xa1[1]); \
  lds_read128<4096>(xa1[0], f1); \
  lds_read128<8192>(wb1[0], f1); \
  lds_read128<6144>(xa1[1], f1); \
  lds_read128<10240>(wb1[1], f1); \
  lds_read128<12288>(wb1[2], f1); \
  lds_read128<14336>(wb1[3], f1); \
  asm volatile("s_waitcnt lgkmcnt(6)" : "+v"(xa0[0]), "+v"(xa0[1]), "+v"(wb0[0]), "+v"(wb0[1]), "+v"(wb0[2]), "+v"(wb0[3]) :: "memory"); \
  mfma_acc(a.acc[0][0], xa0[0], wb0[0]); \
  mfma_acc(a.acc[1][0], xa0[1], wb0[0]); \
  mfma_acc(a.acc[0][1], xa0[0], wb0[1]); \
  dma_piece<91136, 512, 0>(c.rsX, c.vx, c.lds0, c.sx); \
  mfma_acc(a.acc[1][1], xa0[1], wb0[1]); \
  mfma_acc(a.acc[0][2], xa0[0], wb0[2]); \
  mfma_acc(a.acc[1][2], xa0[1], wb0[2]); \
  dma_piece<95232, 29184, 0>(c.rsX, c.vx, c.lds0, c.sx); \
  mfma_acc(a.acc[0][3], xa0[0], wb0[3]); \
  mfma_acc(a.acc[1][3], xa0[1], wb0[3]); \
  mfma_acc(a.aux[0], ones, wb0[3]); \
  dma_piece<99328, 7680, 0>(c.rsW, c.vw, c.lds0, c.sw); \
  mfma_acc(a.aux[1], xa0[0], xa0[0]); \
  mfma_acc(a.aux[2], xa0[1], xa0[1]); \
  asm volatile("s_waitcnt lgkmcnt(0)" : "+v"(xa1[0]), "+v"(xa1[1]), "+v"(wb1[0]), "+v"(wb1[1]), "+v"(wb1[2]), "+v"(wb1[3]) :: "memory"); \
  wait_vm<9>(); \
  barrier(); \
  f0 += 22528; f1 += 22528; \
  lds_read128<4096>(xa0[0], f0); \
  lds_read128<8192>(wb0[0], f0); \
  lds_read128<6144>(xa0[1], f0); \
  lds_read128<10240>(wb0[1], f0); \
  lds_read128<12288>(wb0[2], f0); \
  lds_read128<14336>(wb0[3], f0); \
  mfma_acc(a.acc[0][0], xa1[0], wb1[0]); \
  mfma_acc(a.acc[1][0], xa1[1], wb1[0]); \
  mfma_acc(a.acc[0][1], xa1[0], wb1[1]); \
  dma_piece<103424, 36352, 0>(c.rsW, c.vw, c.lds0, c.sw); \
  mfma_acc(a.acc[1][1], xa1[1], wb1[1]); \
  mfma_acc(a.acc[0][2], xa1[0], wb1[2]); \
  mfma_acc(a.acc[1][2], xa1[1], wb1[2]); \
  dma_piece<107520, 65024, 0>(c.rsW, c.vw, c.lds0, c.sw); \
  mfma_acc(a.acc[0][3], xa1[0], wb1[3]); \
  mfma_acc(a.acc[1][3], xa1[1], wb1[3]); \
  mfma_acc(a.aux[0], ones, wb1[3]); \
  dma_piece<111616, 93696, 0>(c.rsW, c.vw, c.lds0, c.sw); \
  mfma_acc(a.aux[1], xa1[0], xa1[0]); \
  mfma_acc(a.aux[2], xa1[1], xa1[1]); \
  lds_read128<4096>(xa1[0], f1); \
  lds_read128<8192>(wb1[0], f1); \
  lds_read128<6144>(xa1[1], f1); \
  lds_read128<10240>(wb1[1], f1); \
  lds_read128<12288>(wb1[2], f1); \
  lds_read128<14336>(wb1[3], f1); \
  asm volatile("s_waitcnt lgkmcnt(6)" : "+v"(xa0[0]), "+v"(xa0[1]), "+v"(wb0[0]), "+v"(wb0[1]), "+v"(wb0[2]), "+v"(wb0[3]) :: "memory"); \
  mfma_acc(a.acc[0][0], xa0[0], wb0[0]); \
  mfma_acc(a.acc[1][0], xa0[1], wb0[0]); \
  mfma_acc(a.acc[0][1], xa0[0], wb0[1]); \
  dma_piece<113664, 640, 0>(c.rsX, c.vx, c.lds0, c.sx); \
  mfma_acc(a.acc[1][1], xa0[1], wb0[1]); \
  mfma_acc(a.acc[0][2], xa0[0], wb0[2]); \
  mfma_acc(a.acc[1][2], xa0[1], wb0[2]); \
  dma_piece<117760, 29312, 0>(c.rsX, c.vx, c.lds0, c.sx); \
  mfma_acc(a.acc[0][3], xa0[0], wb0[3]); \
  mfma_acc(a.acc[1][3], xa0[1], wb0[3]); \
  mfma_acc(a.aux[0], ones, wb0[3]); \
  dma_piece<121856, 7808, 0>(c.rsW, c.vw, c.lds0, c.sw); \
  mfma_acc(a.aux[1], xa0[0], xa0[0]); \
  mfma_acc(a.aux[2], xa0[1], xa0[1]); \
  asm volatile("s_waitcnt lgkmcnt(0)" : "+v"(xa1[0]), "+v"(xa1[1]), "+v"(wb1[0]), "+v"(wb1[1]), "+v"(wb1[2]), "+v"(wb1[3]) :: "memory"); \
  wait_vm<9>(); \
  barrier(); \
  f0 += 22528; f1 += 22528; \
  lds_read128<4096>(xa0[0], f0); \
  lds_read128<8192>(wb0[0], f0); \
  lds_read128<6144>(xa0[1], f0); \
  lds_read128<10240>(wb0[1], f0); \
  lds_read128<12288>(wb0[2], f0); \
  lds_read128<14336>(wb0[3], f0); \
  mfma_acc(a.acc[0][0], xa1[0], wb1[0]); \
  mfma_acc(a.acc[1][0], xa1[1], wb1[0]); \
  mfma_acc(a.acc[0][1], xa1[0], wb1[1]); \
  dma_piece<125952, 36480, 0>(c.rsW, c.vw, c.lds0, c.sw); \
  mfma_acc(a.acc[1][1], xa1[1], wb1[1]); \
  mfma_acc(a.acc[0][2], xa1[0], wb1[2]); \
  mfma_acc(a.acc[1][2], xa1[1], wb1[2]); \
  dma_piece<130048, 65152, 0>(c.rsW, c.vw, c.lds0, c.sw); \
  mfma_acc(a.acc[0][3], xa1[0], wb1[3]); \
  mfma_acc(a.acc[1][3], xa1[1], wb1[3]); \
  mfma_acc(a.aux[0], ones, wb1[3]); \
  dma_piece<134144, 93824, 0>(c.rsW, c.vw, c.lds0, c.sw); \
  mfma_acc(a.aux[1], xa1[0], xa1[0]); \
  mfma_acc(a.aux[2], xa1[1], xa1[1]); \
  lds_read128<4096>(xa1[0], f1); \
  lds_read128<8192>(wb1[0], f1); \
  lds_read128<6144>(xa1[1], f1); \
  lds_read128<10240>(wb1[1], f1); \
  lds_read128<12288>(wb1[2], f1); \
  lds_read128<14336>(wb1[3], f1); \
  asm volatile("s_waitcnt lgkmcnt(6)" : "+v"(xa0[0]), "+v"(xa0[1]), "+v"(wb0[0]), "+v"(wb0[1]), "+v"(wb0[2]), "+v"(wb0[3]) :: "memory"); \
  mfma_acc(a.acc[0][0], xa0[0], wb0[0]); \
  mfma_acc(a.acc[1][0], xa0[1], wb0[0]); \
  mfma_acc(a.acc[0][1], xa0[0], wb0[1]); \
  dma_piece<136192, 768, 0>(c.rsX, c.vx, c.lds0, c.sx); \
  mfma_acc(a.acc[1][1], xa0[1], wb0[1]); \
  mfma_acc(a.acc[0][2], xa0[0], wb0[2]); \
  mfma_acc(a.acc[1][2], xa0[1], wb0[2]); \
  dma_piece<140288, 29440, 0>(c.rsX, c.vx, c.lds0, c.sx); \
  mfma_acc(a.acc[0][3], xa0[0], wb0[3]); \
  mfma_acc(a.acc[1][3], xa0[1], wb0[3]); \
  mfma_acc(a.aux[0], ones, wb0[3]); \
  dma_piece<144384, 7936, 0>(c.rsW, c.vw, c.lds0, c.sw); \
  mfma_acc(a.aux[1], xa0[0], xa0[0]); \
  mfma_acc(a.aux[2], xa0[1], xa0[1]); \
  asm volatile("s_waitcnt lgkmcnt(0)" : "+v"(xa1[0]), "+v"(xa1[1]), "+v"(wb1[0]), "+v"(wb1[1]), "+v"(wb1[2]), "+v"(wb1[3]) :: "memory"); \
  wait_vm<9>(); \
  barrier(); \
  f0 += 22528; f1 += 22528; \
  lds_read128<4096>(xa0[0], f0); \
  lds_read128<8192>(wb0[0], f0); \
  lds_read128<6144>(xa0[1], f0); \
  lds_read128<10240>(wb0[1], f0); \
  lds_read128<12288>(wb0[2], f0); \
  lds_read128<14336>(wb0[3], f0); \
  mfma_acc(a.acc[0][0], xa1[0], wb1[0]); \
  mfma_acc(a.acc[1][0], xa1[1], wb1[0]); \
  mfma_acc(a.acc[0][1], xa1[0], wb1[1]); \
  dma_piece<148480, 36608, 0>(c.rsW, c.vw, c.lds0, c.sw); \
  mfma_acc(a.acc[1][1], xa1[1], wb1[1]); \
  mfma_acc(a.acc[0][2], xa1[0], wb1[2]); \
  mfma_acc(a.acc[1][2], xa1[1], wb1[2]); \
  dma_piece<152576, 65280, 0>(c.rsW, c.vw, c.lds0, c.sw); \
  mfma_acc(a.acc[0][3], xa1[0], wb1[3]); \
  mfma_acc(a.acc[1][3], xa1[1], wb1[3]); \
  mfma_acc(a.aux[0], ones, wb1[3]); \
  dma_piece<156672, 93952, 0>(c.rsW, c.vw, c.lds0, c.sw); \
  mfma_acc(a.aux[1], xa1[0], xa1[0]); \
  mfma_acc(a.aux[2], xa1[1], xa1[1]); \
  lds_read128<4096>(xa1[0], f1); \
  lds_read128<8192>(wb1[0], f1); \
  lds_read128<6144>(xa1[1], f1); \
  lds_read128<10240>(wb1[1], f1); \
  lds_read128<12288>(wb1[2], f1); \
  lds_read128<14336>(wb1[3], f1); \
  asm volatile("s_waitcnt lgkmcnt(6)" : "+v"(xa0[0]), "+v"(xa0[1]), "+v"(wb0[0]), "+v"(wb0[1]), "+v"(wb0[2]), "+v"(wb0[3]) :: "memory"); \
  mfma_acc(a.acc[0][0], xa0[0], wb0[0]); \
  mfma_acc(a.acc[1][0], xa0[1], wb0[0]); \
  mfma_acc(a.acc[0][1], xa0[0], wb0[1]); \
  dma_piece<0, 0, 1>(A.rsC, A.v, A.ldsw, A.src[0]); \
  mfma_acc(a.acc[1][1], xa0[1], wb0[1]); \
  mfma_acc(a.acc[0][2], xa0[0], wb0[2]); \
  mfma_acc(a.acc[1][2], xa0[1], wb0[2]); \
  dma_piece<2048, 65536, 1>(A.rsC, A.v, A.ldsw, A.src[0]); \
  mfma_acc(a.acc[0][3], xa0[0], wb0[3]); \
  mfma_acc(a.acc[1][3], xa0[1], wb0[3]); \
  mfma_acc(a.aux[0], ones, wb0[3]); \
  dma_piece<4096, 131072, 1>(A.rsC, A.v, A.ldsw, A.src[0]); \
  mfma_acc(a.aux[1], xa0[0], xa0[0]); \
  mfma_acc(a.aux[2], xa0[1], xa0[1]); \
  asm volatile("s_waitcnt lgkmcnt(0)" : "+v"(xa1[0]), "+v"(xa1[1]), "+v"(wb1[0]), "+v"(wb1[1]), "+v"(wb1[2]), "+v"(wb1[3]) :: "memory"); \
  wait_vm<9>(); \
  barrier(); \
  f0 += 22528; f1 += 22528; \
  lds_read128<4096>(xa0[0], f0); \
  lds_read128<8192>(wb0[0], f0); \
  lds_read128<6144>(xa0[1], f0); \
  lds_read128<10240>(wb0[1], f0); \
  lds_read128<12288>(wb0[2], f0); \
  lds_read128<14336>(wb0[3], f0); \
  mfma_acc(a.acc[0][0], xa1[0], wb1[0]); \
  mfma_acc(a.acc[1][0], xa1[1], wb1[0]); \
  mfma_acc(a.acc[0][1], xa1[0], wb1[1]); \
  dma_piece<6144, 196608, 1>(A.rsC, A.v, A.ldsw, A.src[0]); \
  mfma_acc(a.acc[1][1], xa1[1], wb1[1]); \
  mfma_acc(a.acc[0][2], xa1[0], wb1[2]); \
  mfma_acc(a.acc[1][2], xa1[1], wb1[2]); \
  dma_piece<16384, 0, 1>(A.rsC, A.v, A.ldsw, A.src[1]); \
  mfma_acc(a.acc[0][3], xa1[0], wb1[3]); \
  mfma_acc(a.acc[1][3], xa1[1], wb1[3]); \
  mfma_acc(a.aux[0], ones, wb1[3]); \
  dma_piece<18432, 65536, 1>(A.rsC, A.v, A.ldsw, A.src[1]); \
  mfma_acc(a.aux[1], xa1[0], xa1[0]); \
  mfma_acc(a.aux[2], xa1[1], xa1[1]); \
  lds_read128<4096>(xa1[0], f1); \
  lds_read128<8192>(wb1[0], f1); \
  lds_read128<6144>(xa1[1], f1); \
  lds_read128<10240>(wb1[1], f1); \
  lds_read128<12288>(wb1[2], f1); \
  lds_read128<14336>(wb1[3], f1); \
  asm volatile("s_waitcnt lgkmcnt(6)" : "+v"(xa0[0]), "+v"(xa0[1]), "+v"(wb0[0]), "+v"(wb0[1]), "+v"(wb0[2]), "+v"(wb0[3]) :: "memory"); \
  mfma_acc(a.acc[0][0], xa0[0], wb0[0]); \
  mfma_acc(a.acc[1][0], xa0[1], wb0[0]); \
  mfma_acc(a.acc[0][1], xa0[0], wb0[1]); \
  dma_piece<20480, 131072, 1>(A.rsC, A.v, A.ldsw, A.src[1]); \
  mfma_acc(a.acc[1][1], xa0[1], wb0[1]); \
  mfma_acc(a.acc[0][2], xa0[0], wb0[2]); \
  mfma_acc(a.acc[1][2], xa0[1], wb0[2]); \
  dma_piece<22528, 196608, 1>(A.rsC, A.v, A.ldsw, A.src[1]); \
  mfma_acc(a.acc[0][3], xa0[0], wb0[3]); \
  mfma_acc(a.acc[1][3], xa0[1], wb0[3]); \
  mfma_acc(a.aux[0], ones, wb0[3]); \
  dma_piece<32768, 0, 1>(A.rsC, A.v, A.ldsw, A.src[2]); \
  mfma_acc(a.aux[1], xa0[0], xa0[0]); \
  mfma_acc(a.aux[2], xa0[1], xa0[1]); \
  asm volatile("s_waitcnt lgkmcnt(0)" : "+v"(xa1[0]), "+v"(xa1[1]), "+v"(wb1[0]), "+v"(wb1[1]), "+v"(wb1[2]), "+v"(wb1[3]) :: "memory"); \
  wait_vm<9>(); \
  barrier(); \
  f0 += 22528; f1 += 22528; \
  lds_read128<4096>(xa0[0], f0); \
  lds_read128<8192>(wb0[0], f0); \
  lds_read128<6144>(xa0[1], f0); \
  lds_read128<10240>(wb0[1], f0); \
  lds_read128<12288>(wb0[2], f0); \
  lds_read128<14336>(wb0[3], f0); \
  mfma_acc(a.acc[0][0], xa1[0], wb1[0]); \
  mfma_acc(a.acc[1][0], xa1[1], wb1[0]); \
  mfma_acc(a.acc[0][1], xa1[0], wb1[1]); \
  dma_piece<34816, 65536, 1>(A.rsC, A.v, A.ldsw, A.src[2]); \
  mfma_acc(a.acc[1][1], xa1[1], wb1[1]); \
  mfma_acc(a.acc[0][2], xa1[0], wb1[2]); \
  mfma_acc(a.acc[1][2], xa1[1], wb1[2]); \
  dma_piece<36864, 131072, 1>(A.rsC, A.v, A.ldsw, A.src[2]); \
  mfma_acc(a.acc[0][3], xa1[0], wb1[3]); \
  mfma_acc(a.acc[1][3], xa1[1], wb1[3]); \
  mfma_acc(a.aux[0], ones, wb1[3]); \
  dma_piece<38912, 196608, 1>(A.rsC, A.v, A.ldsw, A.src[2]); \
  mfma_acc(a.aux[1], xa1[0], xa1[0]); \
  mfma_acc(a.aux[2], xa1[1], xa1[1]); \
  lds_read128<4096>(xa1[0], f1); \
  lds_read128<8192>(wb1[0], f1); \
  lds_read128<6144>(xa1[1], f1); \
  lds_read128<10240>(wb1[1], f1); \
  lds_read128<12288>(wb1[2], f1); \
  lds_read128<14336>(wb1[3], f1); \
  asm volatile("s_waitcnt lgkmcnt(6)" : "+v"(xa0[0]), "+v"(xa0[1]), "+v"(wb0[0]), "+v"(wb0[1]), "+v"(wb0[2]), "+v"(wb0[3]) :: "memory"); \
  mfma_acc(a.acc[0][0], xa0[0], wb0[0]); \
  mfma_acc(a.acc[1][0], xa0[1], wb0[0]); \
  mfma_acc(a.acc[0][1], xa0[0], wb0[1]); \
  dma_piece<49152, 0, 1>(A.rsC, A.v, A.ldsw, A.src[3]); \
  mfma_acc(a.acc[1][1], xa0[1], wb0[1]); \
  mfma_acc(a.acc[0][2], xa0[0], wb0[2]); \
  mfma_acc(a.acc[1][2], xa0[1], wb0[2]); \
  dma_piece<51200, 65536, 1>(A.rsC, A.v, A.ldsw, A.src[3]); \
  mfma_acc(a.acc[0][3], xa0[0], wb0[3]); \
  mfma_acc(a.acc[1][3], xa0[1], wb0[3]); \
  mfma_acc(a.aux[0], ones, wb0[3]); \
  dma_piece<53248, 131072, 1>(A.rsC, A.v, A.ldsw, A.src[3]); \
  mfma_acc(a.aux[1], xa0[0], xa0[0]); \
  mfma_acc(a.aux[2], xa0[1], xa0[1]); \
  asm volatile("s_waitcnt lgkmcnt(0)" : "+v"(xa1[0]), "+v"(xa1[1]), "+v"(wb1[0]), "+v"(wb1[1]), "+v"(wb1[2]), "+v"(wb1[3]) :: "memory"); \
  mfma_acc(a.acc[0][0], xa1[0], wb1[0]); \
  mfma_acc(a.acc[1][0], xa1[1], wb1[0]); \
  mfma_acc(a.acc[0][1], xa1[0], wb1[1]); \
  dma_piece<55296, 196608, 1>(A.rsC, A.v, A.ldsw, A.src[3]); \
  mfma_acc(a.acc[1][1], xa1[1], wb1[1]); \
  mfma_acc(a.acc[0][2], xa1[0], wb1[2]); \
  mfma_acc(a.acc[1][2], xa1[1], wb1[2]); \
  mfma_acc(a.acc[0][3], xa1[0], wb1[3]); \
  mfma_acc(a.acc[1][3], xa1[1], wb1[3]); \
  mfma_acc(a.aux[0], ones, wb1[3]); \
  mfma_acc(a.aux[1], xa1[0], xa1[0]); \
  mfma_acc(a.aux[2], xa1[1], xa1[1]); \
  P1_FINISH_STATS(); \
  P1_EPILOGUE_COMPUTE(); \
  P1_STORE(0); \
  P1_STORE(1); \
  P1_STORE(2); \
  P1_STORE(3); \
  dma_piece<65536, 0, 1>(A.rsC, A.v, A.ldsw, A.src[4]); \
  dma_piece<67584, 65536, 1>(A.rsC, A.v, A.ldsw, A.src[4]); \
  dma_piece<69632, 131072, 1>(A.rsC, A.v, A.ldsw, A.src[4]); \
  dma_piece<71680, 196608, 1>(A.rsC, A.v, A.ldsw, A.src[4]); \
  wait_vm<4>(); \
  barrier(); \
  P1_FLAG(); \
} while (0)
#define GEN_P1_W2 do { \
  dma_piece<2048, 3584, 0>(c.rsX, c.vx, c.lds0, c.sx); \
  dma_piece<6144, 32256, 0>(c.rsX, c.vx, c.lds0, c.sx); \
  dma_piece<10240, 14336, 0>(c.rsW, c.vw, c.lds0, c.sw); \
  dma_piece<14336, 43008, 0>(c.rsW, c.vw, c.lds0, c.sw); \
  dma_piece<18432, 71680, 0>(c.rsW, c.vw, c.lds0, c.sw); \
  dma_piece<24576, 3712, 0>(c.rsX, c.vx, c.lds0, c.sx); \
  dma_piece<28672, 32384, 0>(c.rsX, c.vx, c.lds0, c.sx); \
  dma_piece<32768, 14464, 0>(c.rsW, c.vw, c.lds0, c.sw); \
  dma_piece<36864, 43136, 0>(c.rsW, c.vw, c.lds0, c.sw); \
  dma_piece<40960, 71808, 0>(c.rsW, c.vw, c.lds0, c.sw); \
  XW_TOUCH(); \
  dma_piece<47104, 3840, 0>(c.rsX, c.vx, c.lds0, c.sx); \
  dma_piece<51200, 32512, 0>(c.rsX, c.vx, c.lds0, c.sx); \
  dma_piece<55296, 14592, 0>(c.rsW, c.vw, c.lds0, c.sw); \
  dma_piece<59392, 43264, 0>(c.rsW, c.vw, c.lds0, c.sw); \
  dma_piece<63488, 71936, 0>(c.rsW, c.vw, c.lds0, c.sw); \
  A_SETUP(); \
  P1_INIT(); \
  wait_vm<11>(); \
  barrier(); \
  lds_read128<0>(xa0[0], f0); \
  lds_read128<16384>(wb0[0], f0); \
  lds_read128<2048>(xa0[1], f0); \
  lds_read128<18432>(wb0[1], f0); \
  lds_read128<20480>(wb0[2], f0); \
  lds_read128<0>(xa1[0], f1); \
  lds_read128<16384>(wb1[0], f1); \
  lds_read128<2048>(xa1[1], f1); \
  lds_read128<18432>(wb1[1], f1); \
  lds_read128<20480>(wb1[2], f1); \
  asm volatile("s_waitcnt lgkmcnt(5)" : "+v"(xa0[0]), "+v"(xa0[1]), "+v"(wb0[0]), "+v"(wb0[1]), "+v"(wb0[2]) :: "memory"); \
  mfma_acc(a.acc[0][0], xa0[0], wb0[0]); \
  mfma_acc(a.acc[1][0], xa0[1], wb0[0]); \
  mfma_acc(a.acc[0][1], xa0[0], wb0[1]); \
  dma_piece<69632, 3968, 0>(c.rsX, c.vx, c.lds0, c.sx); \
  mfma_acc(a.acc[1][1], xa0[1], wb0[1]); \
  mfma_acc(a.acc[0][2], xa0[0], wb0[2]); \
  mfma_acc(a.acc[1][2], xa0[1], wb0[2]); \
  dma_piece<73728, 32640, 0>(c.rsX, c.vx, c.lds0, c.sx); \
  mfma_acc(a.acc[0][3], xa0[0], ones); \
  mfma_acc(a.acc[1][3], xa0[1], ones); \
  mfma_acc(a.aux[0], ones, wb0[0]); \
  dma_piece<77824, 14720, 0>(c.rsW, c.vw, c.lds0, c.sw); \
  mfma_acc(a.aux[1], xa0[0], xa0[0]); \
  mfma_acc(a.aux[2], xa0[1], xa0[1]); \
  asm volatile("s_waitcnt lgkmcnt(0)" : "+v"(xa1[0]), "+v"(xa1[1]), "+v"(wb1[0]), "+v"(wb1[1]), "+v"(wb1[2]) :: "memory"); \
  wait_vm<9>(); \
  barrier(); \
  f0 += 22528; f1 += 22528; \
  lds_read128<0>(xa0[0], f0); \
  lds_read128<16384>(wb0[0], f0); \
  lds_read128<2048>(xa0[1], f0); \
  lds_read128<18432>(wb0[1], f0); \
  lds_read128<20480>(wb0[2], f0); \
  mfma_acc(a.acc[0][0], xa1[0], wb1[0]); \
  mfma_acc(a.acc[1][0], xa1[1], wb1[0]); \
  mfma_acc(a.acc[0][1], xa1[0], wb1[1]); \
  dma_piece<81920, 43392, 0>(c.rsW, c.vw, c.lds0, c.sw); \
  mfma_acc(a.acc[1][1], xa1[1], wb1[1]); \
  mfma_acc(a.acc[0][2], xa1[0], wb1[2]); \
  mfma_acc(a.acc[1][2], xa1[1], wb1[2]); \
  dma_piece<86016, 72064, 0>(c.rsW, c.vw, c.lds0, c.sw); \
  mfma_acc(a.acc[0][3], xa1[0], ones); \
  mfma_acc(a.acc[1][3], xa1[1], ones); \
  mfma_acc(a.aux[0], ones, wb1[0]); \
  dma_piece<92160, 4096, 0>(c.rsX, c.vx, c.lds0, c.sx); \
  mfma_acc(a.aux[1], xa1[0], xa1[0]); \
  mfma_acc(a.aux[2], xa1[1], xa1[1]); \
  lds_read128<0>(xa1[0], f1); \
  lds_read128<16384>(wb1[0], f1); \
  lds_read128<2048>(xa1[1], f1); \
  lds_read128<18432>(wb1[1], f1); \
  lds_read128<20480>(wb1[2], f1); \
  asm volatile("s_waitcnt lgkmcnt(5)" : "+v"(xa0[0]), "+v"(xa0[1]), "+v"(wb0[0]), "+v"(wb0[1]), "+v"(wb0[2]) :: "memory"); \
  mfma_acc(a.acc[0][0], xa0[0], wb0[0]); \
  mfma_acc(a.acc[1][0], xa0[1], wb0[0]); \
  mfma_acc(a.acc[0][1], xa0[0], wb0[1]); \
  dma_piece<96256, 32768, 0>(c.rsX, c.vx, c.lds0, c.sx); \
  mfma_acc(a.acc[1][1], xa0[1], wb0[1]); \
  mfma_acc(a.acc[0][2], xa0[0], wb0[2]); \
  mfma_acc(a.acc[1][2], xa0[1], wb0[2]); \
  dma_piece<100352, 14848, 0>(c.rsW, c.vw, c.lds0, c.sw); \
  mfma_acc(a.acc[0][3], xa0[0], ones); \
  mfma_acc(a.acc[1][3], xa0[1], ones); \
  mfma_acc(a.aux[0], ones, wb0[0]); \
  dma_piece<104448, 43520, 0>(c.rsW, c.vw, c.lds0, c.sw); \
  mfma_acc(a.aux[1], xa0[0], xa0[0]); \
  mfma_acc(a.aux[2], xa0[1], xa0[1]); \
  asm volatile("s_waitcnt lgkmcnt(0)" : "+v"(xa1[0]), "+v"(xa1[1]), "+v"(wb1[0]), "+v"(wb1[1]), "+v"(wb1[2]) :: "memory"); \
  wait_vm<9>(); \
  barrier(); \
  f0 += 22528; f1 += 22528; \
  lds_read128<0>(xa0[0], f0); \
  lds_read128<16384>(wb0[0], f0); \
  lds_read128<2048>(xa0[1], f0); \
  lds_read128<18432>(wb0[1], f0); \
  lds_read128<20480>(wb0[2], f0); \
  mfma_acc(a.acc[0][0], xa1[0], wb1[0]); \
  mfma_acc(a.acc[1][0], xa1[1], wb1[0]); \
  mfma_acc(a.acc[0][1], xa1[0], wb1[1]); \
  dma_piece<108544, 72192, 0>(c.rsW, c.vw, c.lds0, c.sw); \
  mfma_acc(a.acc[1][1], xa1[1], wb1[1]); \
  mfma_acc(a.acc[0][2], xa1[0], wb1[2]); \
  mfma_acc(a.acc[1][2], xa1[1], wb1[2]); \
  dma_piece<114688, 4224, 0>(c.rsX, c.vx, c.lds0, c.sx); \
  mfma_acc(a.acc[0][3], xa1[0], ones); \
  mfma_acc(a.acc[1][3], xa1[1], ones); \
  mfma_acc(a.aux[0], ones, wb1[0]); \
  dma_piece<118784, 32896, 0>(c.rsX, c.vx, c.lds0, c.sx); \
  mfma_acc(a.aux[1], xa1[0], xa1[0]); \
  mfma_acc(a.aux[2], xa1[1], xa1[1]); \
  lds_read128<0>(xa1[0], f1); \
  lds_read128<16384>(wb1[0], f1); \
  lds_read128<2048>(xa1[1], f1); \
  lds_read128<18432>(wb1[1], f1); \
  lds_read128<20480>(wb1[2], f1); \
  asm volatile("s_waitcnt lgkmcnt(5)" : "+v"(xa0[0]), "+v"(xa0[1]), "+v"(wb0[0]), "+v"(wb0[1]), "+v"(wb0[2]) :: "memory"); \
  mfma_acc(a.acc[0][0], xa0[0], wb0[0]); \
  mfma_acc(a.acc[1][0], xa0[1], wb0[0]); \
  mfma_acc(a.acc[0][1], xa0[0], wb0[1]); \
  dma_piece<122880, 14976, 0>(c.rsW, c.vw, c.lds0, c.sw); \
  mfma_acc(a.acc[1][1], xa0[1], wb0[1]); \
  mfma_acc(a.acc[0][2], xa0[0], wb0[2]); \
  mfma_acc(a.acc[1][2], xa0[1], wb0[2]); \
  dma_piece<126976, 43648, 0>(c.rsW, c.vw, c.lds0, c.sw); \
  mfma_acc(a.acc[0][3], xa0[0], ones); \
  mfma_acc(a.acc[1][3], xa0[1], ones); \
  mfma_acc(a.aux[0], ones, wb0[0]); \
  dma_piece<131072, 72320, 0>(c.rsW, c.vw, c.lds0, c.sw); \
  mfma_acc(a.aux[1], xa0[0], xa0[0]); \
  mfma_acc(a.aux[2], xa0[1], xa0[1]); \
  asm volatile("s_waitcnt lgkmcnt(0)" : "+v"(xa1[0]), "+v"(xa1[1]), "+v"(wb1[0]), "+v"(wb1[1]), "+v"(wb1[2]) :: "memory"); \
  wait_vm<10>(); \
  barrier(); \
  f0 += 22528; f1 += 22528; \
  lds_read128<0>(xa0[0], f0); \
  lds_read128<16384>(wb0[0], f0); \
  lds_read128<2048>(xa0[1], f0); \
  lds_read128<18432>(wb0[1], f0); \
  lds_read128<20480>(wb0[2], f0); \
  mfma_acc(a.acc[0][0], xa1[0], wb1[0]); \
  mfma_acc(a.acc[1][0], xa1[1], wb1[0]); \
  mfma_acc(a.acc[0][1], xa1[0], wb1[1]); \
  dma_piece<137216, 4352, 0>(c.rsX, c.vx, c.lds0, c.sx); \
  mfma_acc(a.acc[1][1], xa1[1], wb1[1]); \
  mfma_acc(a.acc[0][2], xa1[0], wb1[2]); \
  mfma_acc(a.acc[1][2], xa1[1], wb1[2]); \
  dma_piece<141312, 33024, 0>(c.rsX, c.vx, c.lds0, c.sx); \
  mfma_acc(a.acc[0][3], xa1[0], ones); \
  mfma_acc(a.acc[1][3], xa1[1], ones); \
  mfma_acc(a.aux[0], ones, wb1[0]); \
  dma_piece<145408, 15104, 0>(c.rsW, c.vw, c.lds0, c.sw); \
  mfma_acc(a.aux[1], xa1[0], xa1[0]); \
  mfma_acc(a.aux[2], xa1[1], xa1[1]); \
  lds_read128<0>(xa1[0], f1); \
  lds_read128<16384>(wb1[0], f1); \
  lds_read128<2048>(xa1[1], f1); \
  lds_read128<18432>(wb1[1], f1); \
  lds_read128<20480>(wb1[2], f1); \
  asm volatile("s_waitcnt lgkmcnt(5)" : "+v"(xa0[0]), "+v"(xa0[1]), "+v"(wb0[0]), "+v"(wb0[1]), "+v"(wb0[2]) :: "memory"); \
  mfma_acc(a.acc[0][0], xa0[0], wb0[0]); \
  mfma_acc(a.acc[1][0], xa0[1], wb0[0]); \
  mfma_acc(a.acc[0][1], xa0[0], wb0[1]); \
  dma_piece<149504, 43776, 0>(c.rsW, c.vw, c.lds0, c.sw); \
  mfma_acc(a.acc[1][1], xa0[1], wb0[1]); \
  mfma_acc(a.acc[0][2], xa0[0], wb0[2]); \
  mfma_acc(a.acc[1][2], xa0[1], wb0[2]); \
  dma_piece<153600, 72448, 0>(c.rsW, c.vw, c.lds0, c.sw); \
  mfma_acc(a.acc[0][3], xa0[0], ones); \
  mfma_acc(a.acc[1][3], xa0[1], ones); \
  mfma_acc(a.aux[0], ones, wb0[0]); \
  dma_piece<0, 0, 1>(A.rsC, A.v, A.ldsw, A.src[0]); \
  mfma_acc(a.aux[1], xa0[0], xa0[0]); \
  mfma_acc(a.aux[2], xa0[1], xa0[1]); \
  asm volatile("s_waitcnt lgkmcnt(0)" : "+v"(xa1[0]), "+v"(xa1[1]), "+v"(wb1[0]), "+v"(wb1[1]), "+v"(wb1[2]) :: "memory"); \
  wait_vm<11>(); \
  barrier(); \
  f0 += 22528; f1 += 22528; \
  lds_read128<0>(xa0[0], f0); \
  lds_read128<16384>(wb0[0], f0); \
  lds_read128<2048>(xa0[1], f0); \
  lds_read128<18432>(wb0[1], f0); \
  lds_read128<20480>(wb0[2], f0); \
  mfma_acc(a.acc[0][0], xa1[0], wb1[0]); \
  mfma_acc(a.acc[1][0], xa1[1], wb1[0]); \
  mfma_acc(a.acc[0][1], xa1[0], wb1[1]); \
  dma_piece<2048, 65536, 1>(A.rsC, A.v, A.ldsw, A.src[0]); \
  mfma_acc(a.acc[1][1], xa1[1], wb1[1]); \
  mfma_acc(a.acc[0][2], xa1[0], wb1[2]); \
  mfma_acc(a.acc[1][2], xa1[1], wb1[2]); \
  dma_piece<4096, 131072, 1>(A.rsC, A.v, A.ldsw, A.src[0]); \
  mfma_acc(a.acc[0][3], xa1[0], ones); \
  mfma_acc(a.acc[1][3], xa1[1], ones); \
  mfma_acc(a.aux[0], ones, wb1[0]); \
  dma_piece<6144, 196608, 1>(A.rsC, A.v, A.ldsw, A.src[0]); \
  mfma_acc(a.aux[1], xa1[0], xa1[0]); \
  mfma_acc(a.aux[2], xa1[1], xa1[1]); \
  lds_read128<0>(xa1[0], f1); \
  lds_read128<16384>(wb1[0], f1); \
  lds_read128<2048>(xa1[1], f1); \
  lds_read128<18432>(wb1[1], f1); \
  lds_read128<20480>(wb1[2], f1); \
  asm volatile("s_waitcnt lgkmcnt(5)" : "+v"(xa0[0]), "+v"(xa0[1]), "+v"(wb0[0]), "+v"(wb0[1]), "+v"(wb0[2]) :: "memory"); \
  mfma_acc(a.acc[0][0], xa0[0], wb0[0]); \
  mfma_acc(a.acc[1][0], xa0[1], wb0[0]); \
  mfma_acc(a.acc[0][1], xa0[0], wb0[1]); \
  dma_piece<16384, 0, 1>(A.rsC, A.v, A.ldsw, A.src[1]); \
  mfma_acc(a.acc[1][1], xa0[1], wb0[1]); \
  mfma_acc(a.acc[0][2], xa0[0], wb0[2]); \
  mfma_acc(a.acc[1][2], xa0[1], wb0[2]); \
  dma_piece<18432, 65536, 1>(A.rsC, A.v, A.ldsw, A.src[1]); \
  mfma_acc(a.acc[0][3], xa0[0], ones); \
  mfma_acc(a.acc[1][3], xa0[1], ones); \
  mfma_acc(a.aux[0], ones, wb0[0]); \
  dma_piece<20480, 131072, 1>(A.rsC, A.v, A.ldsw, A.src[1]); \
  mfma_acc(a.aux[1], xa0[0], xa0[0]); \
  mfma_acc(a.aux[2], xa0[1], xa0[1]); \
  asm volatile("s_waitcnt lgkmcnt(0)" : "+v"(xa1[0]), "+v"(xa1[1]), "+v"(wb1[0]), "+v"(wb1[1]), "+v"(wb1[2]) :: "memory"); \
  wait_vm<12>(); \
  barrier(); \
  f0 += 22528; f1 += 22528; \
  lds_read128<0>(xa0[0], f0); \
  lds_read128<16384>(wb0[0], f0); \
  lds_read128<2048>(xa0[1], f0); \
  lds_read128<18432>(wb0[1], f0); \
  lds_read128<20480>(wb0[2], f0); \
  mfma_acc(a.acc[0][0], xa1[0], wb1[0]); \
  mfma_acc(a.acc[1][0], xa1[1], wb1[0]); \
  mfma_acc(a.acc[0][1], xa1[0], wb1[1]); \
  dma_piece<22528, 196608, 1>(A.rsC, A.v, A.ldsw, A.src[1]); \
  mfma_acc(a.acc[1][1], xa1[1], wb1[1]); \
  mfma_acc(a.acc[0][2], xa1[0], wb1[2]); \
  mfma_acc(a.acc[1][2], xa1[1], wb1[2]); \
  dma_piece<32768, 0, 1>(A.rsC, A.v, A.ldsw, A.src[2]); \
  mfma_acc(a.acc[0][3], xa1[0], ones); \
  mfma_acc(a.acc[1][3], xa1[1], ones); \
  mfma_acc(a.aux[0], ones, wb1[0]); \
  dma_piece<34816, 65536, 1>(A.rsC, A.v, A.ldsw, A.src[2]); \
  mfma_acc(a.aux[1], xa1[0], xa1[0]); \
  mfma_acc(a.aux[2], xa1[1], xa1[1]); \
  lds_read128<0>(xa1[0], f1); \
  lds_read128<16384>(wb1[0], f1); \
  lds_read128<2048>(xa1[1], f1); \
  lds_read128<18432>(wb1[1], f1); \
  lds_read128<20480>(wb1[2], f1); \
  asm volatile("s_waitcnt lgkmcnt(5)" : "+v"(xa0[0]), "+v"(xa0[1]), "+v"(wb0[0]), "+v"(wb0[1]), "+v"(wb0[2]) :: "memory"); \
  mfma_acc(a.acc[0][0], xa0[0], wb0[0]); \
  mfma_acc(a.acc[1][0], xa0[1], wb0[0]); \
  mfma_acc(a.acc[0][1], xa0[0], wb0[1]); \
  dma_piece<36864, 131072, 1>(A.rsC, A.v, A.ldsw, A.src[2]); \
  mfma_acc(a.acc[1][1], xa0[1], wb0[1]); \
  mfma_acc(a.acc[0][2], xa0[0], wb0[2]); \
  mfma_acc(a.acc[1][2], xa0[1], wb0[2]); \
  dma_piece<38912, 196608, 1>(A.rsC, A.v, A.ldsw, A.src[2]); \
  mfma_acc(a.acc[0][3], xa0[0], ones); \
  mfma_acc(a.acc[1][3], xa0[1], ones); \
  mfma_acc(a.aux[0], ones, wb0[0]); \
  dma_piece<49152, 0, 1>(A.rsC, A.v, A.ldsw, A.src[3]); \
  mfma_acc(a.aux[1], xa0[0], xa0[0]); \
  mfma_acc(a.aux[2], xa0[1], xa0[1]); \
  asm volatile("s_waitcnt lgkmcnt(0)" : "+v"(xa1[0]), "+v"(xa1[1]), "+v"(wb1[0]), "+v"(wb1[1]), "+v"(wb1[2]) :: "memory"); \
  wait_vm<13>(); \
  barrier(); \
  f0 += 22528; f1 += 22528; \
  lds_read128<0>(xa0[0], f0); \
  lds_read128<16384>(wb0[0], f0); \
  lds_read128<2048>(xa0[1], f0); \
  lds_read128<18432>(wb0[1], f0); \
  lds_read128<20480>(wb0[2], f0); \
  mfma_acc(a.acc[0][0], xa1[0], wb1[0]); \
  mfma_acc(a.acc[1][0], xa1[1], wb1[0]); \
  mfma_acc(a.acc[0][1], xa1[0], wb1[1]); \
  dma_piece<51200, 65536, 1>(A.rsC, A.v, A.ldsw, A.src[3]); \
  mfma_acc(a.acc[1][1], xa1[1], wb1[1]); \
  mfma_acc(a.acc[0][2], xa1[0], wb1[2]); \
  mfma_acc(a.acc[1][2], xa1[1], wb1[2]); \
  dma_piece<53248, 131072, 1>(A.rsC, A.v, A.ldsw, A.src[3]); \
  mfma_acc(a.acc[0][3], xa1[0], ones); \
  mfma_acc(a.acc[1][3], xa1[1], ones); \
  mfma_acc(a.aux[0], ones, wb1[0]); \
  dma_piece<55296, 196608, 1>(A.rsC, A.v, A.ldsw, A.src[3]); \
  mfma_acc(a.aux[1], xa1[0], xa1[0]); \
  mfma_acc(a.aux[2], xa1[1], xa1[1]); \
  lds_read128<0>(xa1[0], f1); \
  lds_read128<16384>(wb1[0], f1); \
  lds_read128<2048>(xa1[1], f1); \
  lds_read128<18432>(wb1[1], f1); \
  lds_read128<20480>(wb1[2], f1); \
  asm volatile("s_waitcnt lgkmcnt(5)" : "+v"(xa0[0]), "+v"(xa0[1]), "+v"(wb0[0]), "+v"(wb0[1]), "+v"(wb0[2]) :: "memory"); \
  mfma_acc(a.acc[0][0], xa0[0], wb0[0]); \
  mfma_acc(a.acc[1][0], xa0[1], wb0[0]); \
  mfma_acc(a.acc[0][1], xa0[0], wb0[1]); \
  mfma_acc(a.acc[1][1], xa0[1], wb0[1]); \
  mfma_acc(a.acc[0][2], xa0[0], wb0[2]); \
  mfma_acc(a.acc[1][2], xa0[1], wb0[2]); \
  mfma_acc(a.acc[0][3], xa0[0], ones); \
  mfma_acc(a.acc[1][3], xa0[1], ones); \
  mfma_acc(a.aux[0], ones, wb0[0]); \
  mfma_acc(a.aux[1], xa0[0], xa0[0]); \
  mfma_acc(a.aux[2], xa0[1], xa0[1]); \
  asm volatile("s_waitcnt lgkmcnt(0)" : "+v"(xa1[0]), "+v"(xa1[1]), "+v"(wb1[0]), "+v"(wb1[1]), "+v"(wb1[2]) :: "memory"); \
  mfma_acc(a.acc[0][0], xa1[0], wb1[0]); \
  mfma_acc(a.acc[1][0], xa1[1], wb1[0]); \
  mfma_acc(a.acc[0][1], xa1[0], wb1[1]); \
  mfma_acc(a.acc[1][1], xa1[1], wb1[1]); \
  mfma_acc(a.acc[0][2], xa1[0], wb1[2]); \
  mfma_acc(a.acc[1][2], xa1[1], wb1[2]); \
  mfma_acc(a.acc[0][3], xa1[0], ones); \
  mfma_acc(a.acc[1][3], xa1[1], ones); \
  mfma_acc(a.aux[0], ones, wb1[0]); \
  mfma_acc(a.aux[1], xa1[0], xa1[0]); \
  mfma_acc(a.aux[2], xa1[1], xa1[1]); \
  P1_FINISH_STATS(); \
  P1_EPILOGUE_COMPUTE(); \
  P1_STORE(0); \
  P1_STORE(1); \
  P1_STORE(2); \
  dma_piece<65536, 0, 1>(A.rsC, A.v, A.ldsw, A.src[4]); \
  dma_piece<67584, 65536, 1>(A.rsC, A.v, A.ldsw, A.src[4]); \
  dma_piece<69632, 131072, 1>(A.rsC, A.v, A.ldsw, A.src[4]); \
  dma_piece<71680, 196608, 1>(A.rsC, A.v, A.ldsw, A.src[4]); \
  wait_vm<4>(); \
  barrier(); \
  P1_FLAG(); \
} while (0)
#define GEN_P1_W3 do { \
  dma_piece<3072, 3584, 0>(c.rsX, c.vx, c.lds0, c.sx); \
  dma_piece<7168, 32256, 0>(c.rsX, c.vx, c.lds0, c.sx); \
  dma_piece<11264, 21504, 0>(c.rsW, c.vw, c.lds0, c.sw); \
  dma_piece<15360, 50176, 0>(c.rsW, c.vw, c.lds0, c.sw); \
  dma_piece<19456, 78848, 0>(c.rsW, c.vw, c.lds0, c.sw); \
  dma_piece<25600, 3712, 0>(c.rsX, c.vx, c.lds0, c.sx); \
  dma_piece<29696, 32384, 0>(c.rsX, c.vx, c.lds0, c.sx); \
  dma_piece<33792, 21632, 0>(c.rsW, c.vw, c.lds0, c.sw); \
  dma_piece<37888, 50304, 0>(c.rsW, c.vw, c.lds0, c.sw); \
  dma_piece<41984, 78976, 0>(c.rsW, c.vw, c.lds0, c.sw); \
  XW_TOUCH(); \
  dma_piece<48128, 3840, 0>(c.rsX, c.vx, c.lds0, c.sx); \
  dma_piece<52224, 32512, 0>(c.rsX, c.vx, c.lds0, c.sx); \
  dma_piece<56320, 21760, 0>(c.rsW, c.vw, c.lds0, c.sw); \
  dma_piece<60416, 50432, 0>(c.rsW, c.vw, c.lds0, c.sw); \
  dma_piece<64512, 79104, 0>(c.rsW, c.vw, c.lds0, c.sw); \
  A_SETUP(); \
  P1_INIT(); \
  wait_vm<11>(); \
  barrier(); \
  lds_read128<4096>(xa0[0], f0); \
  lds_read128<16384>(wb0[0], f0); \
  lds_read128<6144>(xa0[1], f0); \
  lds_read128<18432>(wb0[1], f0); \
  lds_read128<20480>(wb0[2], f0); \
  lds_read128<4096>(xa1[0], f1); \
  lds_read128<16384>(wb1[0], f1); \
  lds_read128<6144>(xa1[1], f1); \
  lds_read128<18432>(wb1[1], f1); \
  lds_read128<20480>(wb1[2], f1); \
  asm volatile("s_waitcnt lgkmcnt(5)" : "+v"(xa0[0]), "+v"(xa0[1]), "+v"(wb0[0]), "+v"(wb0[1]), "+v"(wb0[2]) :: "memory"); \
  mfma_acc(a.acc[0][0], xa0[0], wb0[0]); \
  mfma_acc(a.acc[1][0], xa0[1], wb0[0]); \
  mfma_acc(a.acc[0][1], xa0[0], wb0[1]); \
  dma_piece<70656, 3968, 0>(c.rsX, c.vx, c.lds0, c.sx); \
  mfma_acc(a.acc[1][1], xa0[1], wb0[1]); \
  mfma_acc(a.acc[0][2], xa0[0], wb0[2]); \
  mfma_acc(a.acc[1][2], xa0[1], wb0[2]); \
  dma_piece<74752, 32640, 0>(c.rsX, c.vx, c.lds0, c.sx); \
  mfma_acc(a.acc[0][3], xa0[0], ones); \
  mfma_acc(a.acc[1][3], xa0[1], ones); \
  mfma_acc(a.aux[0], ones, wb0[1]); \
  dma_piece<78848, 21888, 0>(c.rsW, c.vw, c.lds0, c.sw); \
  mfma_acc(a.aux[1], ones, wb0[2]); \
  asm volatile("s_waitcnt lgkmcnt(0)" : "+v"(xa1[0]), "+v"(xa1[1]), "+v"(wb1[0]), "+v"(wb1[1]), "+v"(wb1[2]) :: "memory"); \
  wait_vm<9>(); \
  barrier(); \
  f0 += 22528; f1 += 22528; \
  lds_read128<4096>(xa0[0], f0); \
  lds_read128<16384>(wb0[0], f0); \
  lds_read128<6144>(xa0[1], f0); \
  lds_read128<18432>(wb0[1], f0); \
  lds_read128<20480>(wb0[2], f0); \
  mfma_acc(a.acc[0][0], xa1[0], wb1[0]); \
  mfma_acc(a.acc[1][0], xa1[1], wb1[0]); \
  mfma_acc(a.acc[0][1], xa1[0], wb1[1]); \
  dma_piece<82944, 50560, 0>(c.rsW, c.vw, c.lds0, c.sw); \
  mfma_acc(a.acc[1][1], xa1[1], wb1[1]); \
  mfma_acc(a.acc[0][2], xa1[0], wb1[2]); \
  mfma_acc(a.acc[1][2], xa1[1], wb1[2]); \
  dma_piece<87040, 79232, 0>(c.rsW, c.vw, c.lds0, c.sw); \
  mfma_acc(a.acc[0][3], xa1[0], ones); \
  mfma_acc(a.acc[1][3], xa1[1], ones); \
  mfma_acc(a.aux[0], ones, wb1[1]); \
  dma_piece<93184, 4096, 0>(c.rsX, c.vx, c.lds0, c.sx); \
  mfma_acc(a.aux[1], ones, wb1[2]); \
  lds_read128<4096>(xa1[0], f1); \
  lds_read128<16384>(wb1[0], f1); \
  lds_read128<6144>(xa1[1], f1); \
  lds_read128<18432>(wb1[1], f1); \
  lds_read128<20480>(wb1[2], f1); \
  asm volatile("s_waitcnt lgkmcnt(5)" : "+v"(xa0[0]), "+v"(xa0[1]), "+v"(wb0[0]), "+v"(wb0[1]), "+v"(wb0[2]) :: "memory"); \
  mfma_acc(a.acc[0][0], xa0[0], wb0[0]); \
  mfma_acc(a.acc[1][0], xa0[1], wb0[0]); \
  mfma_acc(a.acc[0][1], xa0[0], wb0[1]); \
  dma_piece<97280, 32768, 0>(c.rsX, c.vx, c.lds0, c.sx); \
  mfma_acc(a.acc[1][1], xa0[1], wb0[1]); \
  mfma_acc(a.acc[0][2], xa0[0], wb0[2]); \
  mfma_acc(a.acc[1][2], xa0[1], wb0[2]); \
  dma_piece<101376, 22016, 0>(c.rsW, c.vw, c.lds0, c.sw); \
  mfma_acc(a.acc[0][3], xa0[0], ones); \
  mfma_acc(a.acc[1][3], xa0[1], ones); \
  mfma_acc(a.aux[0], ones, wb0[1]); \
  dma_piece<105472, 50688, 0>(c.rsW, c.vw, c.lds0, c.sw); \
  mfma_acc(a.aux[1], ones, wb0[2]); \
  asm volatile("s_waitcnt lgkmcnt(0)" : "+v"(xa1[0]), "+v"(xa1[1]), "+v"(wb1[0]), "+v"(wb1[1]), "+v"(wb1[2]) :: "memory"); \
  wait_vm<9>(); \
  barrier(); \
  f0 += 22528; f1 += 22528; \
  lds_read128<4096>(xa0[0], f0); \
  lds_read128<16384>(wb0[0], f0); \
  lds_read128<6144>(xa0[1], f0); \
  lds_read128<18432>(wb0[1], f0); \
  lds_read128<20480>(wb0[2], f0); \
  mfma_acc(a.acc[0][0], xa1[0], wb1[0]); \
  mfma_acc(a.acc[1][0], xa1[1], wb1[0]); \
  mfma_acc(a.acc[0][1], xa1[0], wb1[1]); \
  dma_piece<109568, 79360, 0>(c.rsW, c.vw, c.lds0, c.sw); \
  mfma_acc(a.acc[1][1], xa1[1], wb1[1]); \
  mfma_acc(a.acc[0][2], xa1[0], wb1[2]); \
  mfma_acc(a.acc[1][2], xa1[1], wb1[2]); \
  dma_piece<115712, 4224, 0>(c.rsX, c.vx, c.lds0, c.sx); \
  mfma_acc(a.acc[0][3], xa1[0], ones); \
  mfma_acc(a.acc[1][3], xa1[1], ones); \
  mfma_acc(a.aux[0], ones, wb1[1]); \
  dma_piece<119808, 32896, 0>(c.rsX, c.vx, c.lds0, c.sx); \
  mfma_acc(a.aux[1], ones, wb1[2]); \
  lds_read128<4096>(xa1[0], f1); \
  lds_read128<16384>(wb1[0], f1); \
  lds_read128<6144>(xa1[1], f1); \
  lds_read128<18432>(wb1[1], f1); \
  lds_read128<20480>(wb1[2], f1); \
  asm volatile("s_waitcnt lgkmcnt(5)" : "+v"(xa0[0]), "+v"(xa0[1]), "+v"(wb0[0]), "+v"(wb0[1]), "+v"(wb0[2]) :: "memory"); \
  mfma_acc(a.acc[0][0], xa0[0], wb0[0]); \
  mfma_acc(a.acc[1][0], xa0[1], wb0[0]); \
  mfma_acc(a.acc[0][1], xa0[0], wb0[1]); \
  dma_piece<123904, 22144, 0>(c.rsW, c.vw, c.lds0, c.sw); \
  mfma_acc(a.acc[1][1], xa0[1], wb0[1]); \
  mfma_acc(a.acc[0][2], xa0[0], wb0[2]); \
  mfma_acc(a.acc[1][2], xa0[1], wb0[2]); \
  dma_piece<128000, 50816, 0>(c.rsW, c.vw, c.lds0, c.sw); \
  mfma_acc(a.acc[0][3], xa0[0], ones); \
  mfma_acc(a.acc[1][3], xa0[1], ones); \
  mfma_acc(a.aux[0], ones, wb0[1]); \
  dma_piece<132096, 79488, 0>(c.rsW, c.vw, c.lds0, c.sw); \
  mfma_acc(a.aux[1], ones, wb0[2]); \
  asm volatile("s_waitcnt lgkmcnt(0)" : "+v"(xa1[0]), "+v"(xa1[1]), "+v"(wb1[0]), "+v"(wb1[1]), "+v"(wb1[2]) :: "memory"); \
  wait_vm<10>(); \
  barrier(); \
  f0 += 22528; f1 += 22528; \
  lds_read128<4096>(xa0[0], f0); \
  lds_read128<16384>(wb0[0], f0); \
  lds_read128<6144>(xa0[1], f0); \
  lds_read128<18432>(wb0[1], f0); \
  lds_read128<20480>(wb0[2], f0); \
  mfma_acc(a.acc[0][0], xa1[0], wb1[0]); \
  mfma_acc(a.acc[1][0], xa1[1], wb1[0]); \
  mfma_acc(a.acc[0][1], xa1[0], wb1[1]); \
  dma_piece<138240, 4352, 0>(c.rsX, c.vx, c.lds0, c.sx); \
  mfma_acc(a.acc[1][1], xa1[1], wb1[1]); \
  mfma_acc(a.acc[0][2], xa1[0], wb1[2]); \
  mfma_acc(a.acc[1][2], xa1[1], wb1[2]); \
  dma_piece<142336, 33024, 0>(c.rsX, c.vx, c.lds0, c.sx); \
  mfma_acc(a.acc[0][3], xa1[0], ones); \
  mfma_acc(a.acc[1][3], xa1[1], ones); \
  mfma_acc(a.aux[0], ones, wb1[1]); \
  dma_piece<146432, 22272, 0>(c.rsW, c.vw, c.lds0, c.sw); \
  mfma_acc(a.aux[1], ones, wb1[2]); \
  lds_read128<4096>(xa1[0], f1); \
  lds_read128<16384>(wb1[0], f1); \
  lds_read128<6144>(xa1[1], f1); \
  lds_read128<18432>(wb1[1], f1); \
  lds_read128<20480>(wb1[2], f1); \
  asm volatile("s_waitcnt lgkmcnt(5)" : "+v"(xa0[0]), "+v"(xa0[1]), "+v"(wb0[0]), "+v"(wb0[1]), "+v"(wb0[2]) :: "memory"); \
  mfma_acc(a.acc[0][0], xa0[0], wb0[0]); \
  mfma_acc(a.acc[1][0], xa0[1], wb0[0]); \
  mfma_acc(a.acc[0][1], xa0[0], wb0[1]); \
  dma_piece<150528, 50944, 0>(c.rsW, c.vw, c.lds0, c.sw); \
  mfma_acc(a.acc[1][1], xa0[1], wb0[1]); \
  mfma_acc(a.acc[0][2], xa0[0], wb0[2]); \
  mfma_acc(a.acc[1][2], xa0[1], wb0[2]); \
  dma_piece<154624, 79616, 0>(c.rsW, c.vw, c.lds0, c.sw); \
  mfma_acc(a.acc[0][3], xa0[0], ones); \
  mfma_acc(a.acc[1][3], xa0[1], ones); \
  mfma_acc(a.aux[0], ones, wb0[1]); \
  dma_piece<0, 0, 1>(A.rsC, A.v, A.ldsw, A.src[0]); \
  mfma_acc(a.aux[1], ones, wb0[2]); \
  asm volatile("s_waitcnt lgkmcnt(0)" : "+v"(xa1[0]), "+v"(xa1[1]), "+v"(wb1[0]), "+v"(wb1[1]), "+v"(wb1[2]) :: "memory"); \
  wait_vm<11>(); \
  barrier(); \
  f0 += 22528; f1 += 22528; \
  lds_read128<4096>(xa0[0], f0); \
  lds_read128<16384>(wb0[0], f0); \
  lds_read128<6144>(xa0[1], f0); \
  lds_read128<18432>(wb0[1], f0); \
  lds_read128<20480>(wb0[2], f0); \
  mfma_acc(a.acc[0][0], xa1[0], wb1[0]); \
  mfma_acc(a.acc[1][0], xa1[1], wb1[0]); \
  mfma_acc(a.acc[0][1], xa1[0], wb1[1]); \
  dma_piece<2048, 65536, 1>(A.rsC, A.v, A.ldsw, A.src[0]); \
  mfma_acc(a.acc[1][1], xa1[1], wb1[1]); \
  mfma_acc(a.acc[0][2], xa1[0], wb1[2]); \
  mfma_acc(a.acc[1][2], xa1[1], wb1[2]); \
  dma_piece<4096, 131072, 1>(A.rsC, A.v, A.ldsw, A.src[0]); \
  mfma_acc(a.acc[0][3], xa1[0], ones); \
  mfma_acc(a.acc[1][3], xa1[1], ones); \
  mfma_acc(a.aux[0], ones, wb1[1]); \
  dma_piece<6144, 196608, 1>(A.rsC, A.v, A.ldsw, A.src[0]); \
  mfma_acc(a.aux[1], ones, wb1[2]); \
  lds_read128<4096>(xa1[0], f1); \
  lds_read128<16384>(wb1[0], f1); \
  lds_read128<6144>(xa1[1], f1); \
  lds_read128<18432>(wb1[1], f1); \
  lds_read128<20480>(wb1[2], f1); \
  asm volatile("s_waitcnt lgkmcnt(5)" : "+v"(xa0[0]), "+v"(xa0[1]), "+v"(wb0[0]), "+v"(wb0[1]), "+v"(wb0[2]) :: "memory"); \
  mfma_acc(a.acc[0][0], xa0[0], wb0[0]); \
  mfma_acc(a.acc[1][0], xa0[1], wb0[0]); \
  mfma_acc(a.acc[0][1], xa0[0], wb0[1]); \
  dma_piece<16384, 0, 1>(A.rsC, A.v, A.ldsw, A.src[1]); \
  mfma_acc(a.acc[1][1], xa0[1], wb0[1]); \
  mfma_acc(a.acc[0][2], xa0[0], wb0[2]); \
  mfma_acc(a.acc[1][2], xa0[1], wb0[2]); \
  dma_piece<18432, 65536, 1>(A.rsC, A.v, A.ldsw, A.src[1]); \
  mfma_acc(a.acc[0][3], xa0[0], ones); \
  mfma_acc(a.acc[1][3], xa0[1], ones); \
  mfma_acc(a.aux[0], ones, wb0[1]); \
  dma_piece<20480, 131072, 1>(A.rsC, A.v, A.ldsw, A.src[1]); \
  mfma_acc(a.aux[1], ones, wb0[2]); \
  asm volatile("s_waitcnt lgkmcnt(0)" : "+v"(xa1[0]), "+v"(xa1[1]), "+v"(wb1[0]), "+v"(wb1[1]), "+v"(wb1[2]) :: "memory"); \
  wait_vm<12>(); \
  barrier(); \
  f0 += 22528; f1 += 22528; \
  lds_read128<4096>(xa0[0], f0); \
  lds_read128<16384>(wb0[0], f0); \
  lds_read128<6144>(xa0[1], f0); \
  lds_read128<18432>(wb0[1], f0); \
  lds_read128<20480>(wb0[2], f0); \
  mfma_acc(a.acc[0][0], xa1[0], wb1[0]); \
  mfma_acc(a.acc[1][0], xa1[1], wb1[0]); \
  mfma_acc(a.acc[0][1], xa1[0], wb1[1]); \
  dma_piece<22528, 196608, 1>(A.rsC, A.v, A.ldsw, A.src[1]); \
  mfma_acc(a.acc[1][1], xa1[1], wb1[1]); \
  mfma_acc(a.acc[0][2], xa1[0], wb1[2]); \
  mfma_acc(a.acc[1][2], xa1[1], wb1[2]); \
  dma_piece<32768, 0, 1>(A.rsC, A.v, A.ldsw, A.src[2]); \
  mfma_acc(a.acc[0][3], xa1[0], ones); \
  mfma_acc(a.acc[1][3], xa1[1], ones); \
  mfma_acc(a.aux[0], ones, wb1[1]); \
  dma_piece<34816, 65536, 1>(A.rsC, A.v, A.ldsw, A.src[2]); \
  mfma_acc(a.aux[1], ones, wb1[2]); \
  lds_read128<4096>(xa1[0], f1); \
  lds_read128<16384>(wb1[0], f1); \
  lds_read128<6144>(xa1[1], f1); \
  lds_read128<18432>(wb1[1], f1); \
  lds_read128<20480>(wb1[2], f1); \
  asm volatile("s_waitcnt lgkmcnt(5)" : "+v"(xa0[0]), "+v"(xa0[1]), "+v"(wb0[0]), "+v"(wb0[1]), "+v"(wb0[2]) :: "memory"); \
  mfma_acc(a.acc[0][0], xa0[0], wb0[0]); \
  mfma_acc(a.acc[1][0], xa0[1], wb0[0]); \
  mfma_acc(a.acc[0][1], xa0[0], wb0[1]); \
  dma_piece<36864, 131072, 1>(A.rsC, A.v, A.ldsw, A.src[2]); \
  mfma_acc(a.acc[1][1], xa0[1], wb0[1]); \
  mfma_acc(a.acc[0][2], xa0[0], wb0[2]); \
  mfma_acc(a.acc[1][2], xa0[1], wb0[2]); \
  dma_piece<38912, 196608, 1>(A.rsC, A.v, A.ldsw, A.src[2]); \
  mfma_acc(a.acc[0][3], xa0[0], ones); \
  mfma_acc(a.acc[1][3], xa0[1], ones); \
  mfma_acc(a.aux[0], ones, wb0[1]); \
  dma_piece<49152, 0, 1>(A.rsC, A.v, A.ldsw, A.src[3]); \
  mfma_acc(a.aux[1], ones, wb0[2]); \
  asm volatile("s_waitcnt lgkmcnt(0)" : "+v"(xa1[0]), "+v"(xa1[1]), "+v"(wb1[0]), "+v"(wb1[1]), "+v"(wb1[2]) :: "memory"); \
  wait_vm<13>(); \
  barrier(); \
  f0 += 22528; f1 += 22528; \
  lds_read128<4096>(xa0[0], f0); \
  lds_read128<16384>(wb0[0], f0); \
  lds_read128<6144>(xa0[1], f0); \
  lds_read128<18432>(wb0[1], f0); \
  lds_read128<20480>(wb0[2], f0); \
  mfma_acc(a.acc[0][0], xa1[0], wb1[0]); \
  mfma_acc(a.acc[1][0], xa1[1], wb1[0]); \
  mfma_acc(a.acc[0][1], xa1[0], wb1[1]); \
  dma_piece<51200, 65536, 1>(A.rsC, A.v, A.ldsw, A.src[3]); \
  mfma_acc(a.acc[1][1], xa1[1], wb1[1]); \
  mfma_acc(a.acc[0][2], xa1[0], wb1[2]); \
  mfma_acc(a.acc[1][2], xa1[1], wb1[2]); \
  dma_piece<53248, 131072, 1>(A.rsC, A.v, A.ldsw, A.src[3]); \
  mfma_acc(a.acc[0][3], xa1[0], ones); \
  mfma_acc(a.acc[1][3], xa1[1], ones); \
  mfma_acc(a.aux[0], ones, wb1[1]); \
  dma_piece<55296, 196608, 1>(A.rsC, A.v, A.ldsw, A.src[3]); \
  mfma_acc(a.aux[1], ones, wb1[2]); \
  lds_read128<4096>(xa1[0], f1); \
  lds_read128<16384>(wb1[0], f1); \
  lds_read128<6144>(xa1[1], f1); \
  lds_read128<18432>(wb1[1], f1); \
  lds_read128<20480>(wb1[2], f1); \
  asm volatile("s_waitcnt lgkmcnt(5)" : "+v"(xa0[0]), "+v"(xa0[1]), "+v"(wb0[0]), "+v"(wb0[1]), "+v"(wb0[2]) :: "memory"); \
  mfma_acc(a.acc[0][0], xa0[0], wb0[0]); \
  mfma_acc(a.acc[1][0], xa0[1], wb0[0]); \
  mfma_acc(a.acc[0][1], xa0[0], wb0[1]); \
  mfma_acc(a.acc[1][1], xa0[1], wb0[1]); \
  mfma_acc(a.acc[0][2], xa0[0], wb0[2]); \
  mfma_acc(a.acc[1][2], xa0[1], wb0[2]); \
  mfma_acc(a.acc[0][3], xa0[0], ones); \
  mfma_acc(a.acc[1][3], xa0[1], ones); \
  mfma_acc(a.aux[0], ones, wb0[1]); \
  mfma_acc(a.aux[1], ones, wb0[2]); \
  asm volatile("s_waitcnt lgkmcnt(0)" : "+v"(xa1[0]), "+v"(xa1[1]), "+v"(wb1[0]), "+v"(wb1[1]), "+v"(wb1[2]) :: "memory"); \
  mfma_acc(a.acc[0][0], xa1[0], wb1[0]); \
  mfma_acc(a.acc[1][0], xa1[1], wb1[0]); \
  mfma_acc(a.acc[0][1], xa1[0], wb1[1]); \
  mfma_acc(a.acc[1][1], xa1[1], wb1[1]); \
  mfma_acc(a.acc[0][2], xa1[0], wb1[2]); \
  mfma_acc(a.acc[1][2], xa1[1], wb1[2]); \
  mfma_acc(a.acc[0][3], xa1[0], ones); \
  mfma_acc(a.acc[1][3], xa1[1], ones); \
  mfma_acc(a.aux[0], ones, wb1[1]); \
  mfma_acc(a.aux[1], ones, wb1[2]); \
  P1_FINISH_STATS(); \
  P1_EPILOGUE_COMPUTE(); \
  P1_STORE(0); \
  P1_STORE(1); \
  P1_STORE(2); \
  dma_piece<65536, 0, 1>(A.rsC, A.v, A.ldsw, A.src[4]); \
  dma_piece<67584, 65536, 1>(A.rsC, A.v, A.ldsw, A.src[4]); \
  dma_piece<69632, 131072, 1>(A.rsC, A.v, A.ldsw, A.src[4]); \
  dma_piece<71680, 196608, 1>(A.rsC, A.v, A.ldsw, A.src[4]); \
  wait_vm<4>(); \
  barrier(); \
  P1_FLAG(); \
} while (0)
#define GEN_COMMON do { \
  wait_vm<12>(); \
  barrier(); \
  a_read<0, 0>(A.r0); \
  a_read<4, 2048>(A.r0); \
  a_read<8, 4096>(A.r0); \
  a_read<12, 6144>(A.r0); \
  a_read<16, 0>(A.r1); \
  a_read<20, 2048>(A.r1); \
  a_read<24, 4096>(A.r1); \
  a_read<28, 6144>(A.r1); \
  a_read<32, 16384>(A.r0); \
  a_read<36, 18432>(A.r0); \
  a_read<40, 20480>(A.r0); \
  a_read<44, 22528>(A.r0); \
  a_read<48, 16384>(A.r1); \
  a_read<52, 18432>(A.r1); \
  a_read<56, 20480>(A.r1); \
  a_read<60, 22528>(A.r1); \
  dma_piece<81920, 0, 1>(A.rsC, A.v, A.ldsw, A.src[5]); \
  dma_piece<83968, 65536, 1>(A.rsC, A.v, A.ldsw, A.src[5]); \
  dma_piece<86016, 131072, 1>(A.rsC, A.v, A.ldsw, A.src[5]); \
  dma_piece<88064, 196608, 1>(A.rsC, A.v, A.ldsw, A.src[5]); \
  POLL_ISSUE(); \
  wait_vm<9>(); \
  barrier(); \
  a_read<64, 32768>(A.r0); \
  a_read<68, 34816>(A.r0); \
  a_read<72, 36864>(A.r0); \
  a_read<76, 38912>(A.r0); \
  a_read<80, 32768>(A.r1); \
  a_read<84, 34816>(A.r1); \
  a_read<88, 36864>(A.r1); \
  a_read<92, 38912>(A.r1); \
  a_read<96, 49152>(A.r0); \
  a_read<100, 51200>(A.r0); \
  a_read<104, 53248>(A.r0); \
  a_read<108, 55296>(A.r0); \
  a_read<112, 49152>(A.r1); \
  a_read<116, 51200>(A.r1); \
  a_read<120, 53248>(A.r1); \
  a_read<124, 55296>(A.r1); \
  dma_piece<98304, 0, 1>(A.rsC, A.v, A.ldsw, A.src[6]); \
  dma_piece<100352, 65536, 1>(A.rsC, A.v, A.ldsw, A.src[6]); \
  dma_piece<102400, 131072, 1>(A.rsC, A.v, A.ldsw, A.src[6]); \
  dma_piece<104448, 196608, 1>(A.rsC, A.v, A.ldsw, A.src[6]); \
  dma_piece<114688, 0, 1>(A.rsC, A.v, A.ldsw, A.src[7]); \
  dma_piece<116736, 65536, 1>(A.rsC, A.v, A.ldsw, A.src[7]); \
  dma_piece<118784, 131072, 1>(A.rsC, A.v, A.ldsw, A.src[7]); \
  dma_piece<120832, 196608, 1>(A.rsC, A.v, A.ldsw, A.src[7]); \
  wait_vm<9>(); \
  barrier(); \
  a_read<128, 0>(A.r0h); \
  a_read<132, 2048>(A.r0h); \
  a_read<136, 4096>(A.r0h); \
  a_read<140, 6144>(A.r0h); \
  a_read<144, 0>(A.r1h); \
  a_read<148, 2048>(A.r1h); \
  a_read<152, 4096>(A.r1h); \
  a_read<156, 6144>(A.r1h); \
  a_read<160, 16384>(A.r0h); \
  a_read<164, 18432>(A.r0h); \
  a_read<168, 20480>(A.r0h); \
  a_read<172, 22528>(A.r0h); \
  a_read<176, 16384>(A.r1h); \
  a_read<180, 18432>(A.r1h); \
  a_read<184, 20480>(A.r1h); \
  a_read<188, 22528>(A.r1h); \
  wait_vm<8>(); \
  POLL_CHECK(); \
  b_load<-3072>(c.b[0], vlane, B_STEP(0)); \
  b_load<-2048>(c.b[1], vlane, B_STEP(0)); \
  b_load<-1024>(c.b[2], vlane, B_STEP(0)); \
  b_load<0>(c.b[3], vlane, B_STEP(0)); \
  b_load<1024>(c.b[4], vlane, B_STEP(0)); \
  b_load<2048>(c.b[5], vlane, B_STEP(0)); \
  b_load<3072>(c.b[6], vlane, B_STEP(0)); \
  b_load<-3072>(c.b[7], vlane, B_STEP(1)); \
  b_load<-2048>(c.b[8], vlane, B_STEP(1)); \
  b_load<-1024>(c.b[9], vlane, B_STEP(1)); \
  b_load<0>(c.b[10], vlane, B_STEP(1)); \
  b_load<1024>(c.b[11], vlane, B_STEP(1)); \
  wait_lgkm<0>(); \
  grp<1, 11, 0>(c.acc[0][0], c.acc[1][0], c.acc[2][0], c.acc[3][0], c.b[0]); \
  b_load<2048>(c.b[0], vlane, B_STEP(1)); \
  grp<1, 11, 0>(c.acc[0][1], c.acc[1][1], c.acc[2][1], c.acc[3][1], c.b[1]); \
  b_load<3072>(c.b[1], vlane, B_STEP(1)); \
  barrier(); \
  grp<1, 11, 0>(c.acc[0][2], c.acc[1][2], c.acc[2][2], c.acc[3][2], c.b[2]); \
  b_load<-3072>(c.b[2], vlane, B_STEP(2)); \
  a_read<192, 32768>(A.r0h); \
  grp<1, 11, 0>(c.acc[0][3], c.acc[1][3], c.acc[2][3], c.acc[3][3], c.b[3]); \
  b_load<-2048>(c.b[3], vlane, B_STEP(2)); \
  a_read<196, 34816>(A.r0h); \
  grp<1, 11, 0>(c.acc[0][4], c.acc[1][4], c.acc[2][4], c.acc[3][4], c.b[4]); \
  b_load<-1024>(c.b[4], vlane, B_STEP(2)); \
  a_read<200, 36864>(A.r0h); \
  grp<1, 11, 0>(c.acc[0][5], c.acc[1][5], c.acc[2][5], c.acc[3][5], c.b[5]); \
  b_load<0>(c.b[5], vlane, B_STEP(2)); \
  a_read<204, 38912>(A.r0h); \
  grp<1, 11, 0>(c.acc[0][6], c.acc[1][6], c.acc[2][6], c.acc[3][6], c.b[6]); \
  b_load<1024>(c.b[6], vlane, B_STEP(2)); \
  a_read<208, 32768>(A.r1h); \
  grp<0, 11, 16>(c.acc[0][0], c.acc[1][0], c.acc[2][0], c.acc[3][0], c.b[7]); \
  b_load<2048>(c.b[7], vlane, B_STEP(2)); \
  a_read<212, 34816>(A.r1h); \
  grp<0, 11, 16>(c.acc[0][1], c.acc[1][1], c.acc[2][1], c.acc[3][1], c.b[8]); \
  b_load<3072>(c.b[8], vlane, B_STEP(2)); \
  a_read<216, 36864>(A.r1h); \
  grp<0, 11, 16>(c.acc[0][2], c.acc[1][2], c.acc[2][2], c.acc[3][2], c.b[9]); \
  b_load<-3072>(c.b[9], vlane, B_STEP(3)); \
  a_read<220, 38912>(A.r1h); \
  grp<0, 11, 16>(c.acc[0][3], c.acc[1][3], c.acc[2][3], c.acc[3][3], c.b[10]); \
  b_load<-2048>(c.b[10], vlane, B_STEP(3)); \
  a_read<224, 49152>(A.r0h); \
  grp<0, 11, 16>(c.acc[0][4], c.acc[1][4], c.acc[2][4], c.acc[3][4], c.b[11]); \
  b_load<-1024>(c.b[11], vlane, B_STEP(3)); \
  a_read<228, 51200>(A.r0h); \
  grp<0, 11, 16>(c.acc[0][5], c.acc[1][5], c.acc[2][5], c.acc[3][5], c.b[0]); \
  b_load<0>(c.b[0], vlane, B_STEP(3)); \
  a_read<232, 53248>(A.r0h); \
  grp<0, 11, 16>(c.acc[0][6], c.acc[1][6], c.acc[2][6], c.acc[3][6], c.b[1]); \
  b_load<1024>(c.b[1], vlane, B_STEP(3)); \
  a_read<236, 55296>(A.r0h); \
  grp<0, 11, 32>(c.acc[0][0], c.acc[1][0], c.acc[2][0], c.acc[3][0], c.b[2]); \
  b_load<2048>(c.b[2], vlane, B_STEP(3)); \
  a_read<240, 49152>(A.r1h); \
  grp<0, 11, 32>(c.acc[0][1], c.acc[1][1], c.acc[2][1], c.acc[3][1], c.b[3]); \
  b_load<3072>(c.b[3], vlane, B_STEP(3)); \
  a_read<244, 51200>(A.r1h); \
  grp<0, 11, 32>(c.acc[0][2], c.acc[1][2], c.acc[2][2], c.acc[3][2], c.b[4]); \
  b_load<-3072>(c.b[4], vlane, B_STEP(4)); \
  a_read<248, 53248>(A.r1h); \
  grp<0, 11, 32>(c.acc[0][3], c.acc[1][3], c.acc[2][3], c.acc[3][3], c.b[5]); \
  b_load<-2048>(c.b[5], vlane, B_STEP(4)); \
  a_read<252, 55296>(A.r1h); \
  grp<0, 11, 32>(c.acc[0][4], c.acc[1][4], c.acc[2][4], c.acc[3][4], c.b[6]); \
  b_load<-1024>(c.b[6], vlane, B_STEP(4)); \
  grp<0, 11, 32>(c.acc[0][5], c.acc[1][5], c.acc[2][5], c.acc[3][5], c.b[7]); \
  b_load<0>(c.b[7], vlane, B_STEP(4)); \
  grp<0, 11, 32>(c.acc[0][6], c.acc[1][6], c.acc[2][6], c.acc[3][6], c.b[8]); \
  b_load<1024>(c.b[8], vlane, B_STEP(4)); \
  grp<0, 11, 48>(c.acc[0][0], c.acc[1][0], c.acc[2][0], c.acc[3][0], c.b[9]); \
  b_load<2048>(c.b[9], vlane, B_STEP(4)); \
  grp<0, 11, 48>(c.acc[0][1], c.acc[1][1], c.acc[2][1], c.acc[3][1], c.b[10]); \
  b_load<3072>(c.b[10], vlane, B_STEP(4)); \
  grp<0, 11, 48>(c.acc[0][2], c.acc[1][2], c.acc[2][2], c.acc[3][2], c.b[11]); \
  b_load<-3072>(c.b[11], vlane, B_STEP(5)); \
  grp<0, 11, 48>(c.acc[0][3], c.acc[1][3], c.acc[2][3], c.acc[3][3], c.b[0]); \
  b_load<-2048>(c.b[0], vlane, B_STEP(5)); \
  grp<0, 11, 48>(c.acc[0][4], c.acc[1][4], c.acc[2][4], c.acc[3][4], c.b[1]); \
  b_load<-1024>(c.b[1], vlane, B_STEP(5)); \
  grp<0, 11, 48>(c.acc[0][5], c.acc[1][5], c.acc[2][5], c.acc[3][5], c.b[2]); \
  b_load<0>(c.b[2], vlane, B_STEP(5)); \
  grp<0, 11, 48>(c.acc[0][6], c.acc[1][6], c.acc[2][6], c.acc[3][6], c.b[3]); \
  b_load<1024>(c.b[3], vlane, B_STEP(5)); \
  grp<0, 11, 64>(c.acc[0][0], c.acc[1][0], c.acc[2][0], c.acc[3][0], c.b[4]); \
  b_load<2048>(c.b[4], vlane, B_STEP(5)); \
  grp<0, 11, 64>(c.acc[0][1], c.acc[1][1], c.acc[2][1], c.acc[3][1], c.b[5]); \
  b_load<3072>(c.b[5], vlane, B_STEP(5)); \
  grp<0, 11, 64>(c.acc[0][2], c.acc[1][2], c.acc[2][2], c.acc[3][2], c.b[6]); \
  b_load<-3072>(c.b[6], vlane, B_STEP(6)); \
  grp<0, 11, 64>(c.acc[0][3], c.acc[1][3], c.acc[2][3], c.acc[3][3], c.b[7]); \
  b_load<-2048>(c.b[7], vlane, B_STEP(6)); \
  grp<0, 11, 64>(c.acc[0][4], c.acc[1][4], c.acc[2][4], c.acc[3][4], c.b[8]); \
  b_load<-1024>(c.b[8], vlane, B_STEP(6)); \
  grp<0, 11, 64>(c.acc[0][5], c.acc[1][5], c.acc[2][5], c.acc[3][5], c.b[9]); \
  b_load<0>(c.b[9], vlane, B_STEP(6)); \
  grp<0, 11, 64>(c.acc[0][6], c.acc[1][6], c.acc[2][6], c.acc[3][6], c.b[10]); \
  b_load<1024>(c.b[10], vlane, B_STEP(6)); \
  grp<0, 11, 80>(c.acc[0][0], c.acc[1][0], c.acc[2][0], c.acc[3][0], c.b[11]); \
  b_load<2048>(c.b[11], vlane, B_STEP(6)); \
  grp<0, 11, 80>(c.acc[0][1], c.acc[1][1], c.acc[2][1], c.acc[3][1], c.b[0]); \
  b_load<3072>(c.b[0], vlane, B_STEP(6)); \
  grp<0, 11, 80>(c.acc[0][2], c.acc[1][2], c.acc[2][2], c.acc[3][2], c.b[1]); \
  b_load<-3072>(c.b[1], vlane, B_STEP(7)); \
  grp<0, 11, 80>(c.acc[0][3], c.acc[1][3], c.acc[2][3], c.acc[3][3], c.b[2]); \
  b_load<-2048>(c.b[2], vlane, B_STEP(7)); \
  grp<0, 11, 80>(c.acc[0][4], c.acc[1][4], c.acc[2][4], c.acc[3][4], c.b[3]); \
  b_load<-1024>(c.b[3], vlane, B_STEP(7)); \
  grp<0, 11, 80>(c.acc[0][5], c.acc[1][5], c.acc[2][5], c.acc[3][5], c.b[4]); \
  b_load<0>(c.b[4], vlane, B_STEP(7)); \
  grp<0, 11, 80>(c.acc[0][6], c.acc[1][6], c.acc[2][6], c.acc[3][6], c.b[5]); \
  b_load<1024>(c.b[5], vlane, B_STEP(7)); \
  grp<0, 11, 96>(c.acc[0][0], c.acc[1][0], c.acc[2][0], c.acc[3][0], c.b[6]); \
  b_load<2048>(c.b[6], vlane, B_STEP(7)); \
  grp<0, 11, 96>(c.acc[0][1], c.acc[1][1], c.acc[2][1], c.acc[3][1], c.b[7]); \
  b_load<3072>(c.b[7], vlane, B_STEP(7)); \
  grp<0, 11, 96>(c.acc[0][2], c.acc[1][2], c.acc[2][2], c.acc[3][2], c.b[8]); \
  b_load<-3072>(c.b[8], vlane, B_STEP(8)); \
  grp<0, 11, 96>(c.acc[0][3], c.acc[1][3], c.acc[2][3], c.acc[3][3], c.b[9]); \
  b_load<-2048>(c.b[9], vlane, B_STEP(8)); \
  grp<0, 11, 96>(c.acc[0][4], c.acc[1][4], c.acc[2][4], c.acc[3][4], c.b[10]); \
  b_load<-1024>(c.b[10], vlane, B_STEP(8)); \
  grp<0, 11, 96>(c.acc[0][5], c.acc[1][5], c.acc[2][5], c.acc[3][5], c.b[11]); \
  b_load<0>(c.b[11], vlane, B_STEP(8)); \
  grp<0, 11, 96>(c.acc[0][6], c.acc[1][6], c.acc[2][6], c.acc[3][6], c.b[0]); \
  b_load<1024>(c.b[0], vlane, B_STEP(8)); \
  grp<0, 11, 112>(c.acc[0][0], c.acc[1][0], c.acc[2][0], c.acc[3][0], c.b[1]); \
  b_load<2048>(c.b[1], vlane, B_STEP(8)); \
  grp<0, 11, 112>(c.acc[0][1], c.acc[1][1], c.acc[2][1], c.acc[3][1], c.b[2]); \
  b_load<3072>(c.b[2], vlane, B_STEP(8)); \
  wait_lgkm<0>(); \
  grp<0, 11, 112>(c.acc[0][2], c.acc[1][2], c.acc[2][2], c.acc[3][2], c.b[3]); \
  b_load<-3072>(c.b[3], vlane, B_STEP(9)); \
  grp<0, 11, 112>(c.acc[0][3], c.acc[1][3], c.acc[2][3], c.acc[3][3], c.b[4]); \
  b_load<-2048>(c.b[4], vlane, B_STEP(9)); \
  grp<0, 11, 112>(c.acc[0][4], c.acc[1][4], c.acc[2][4], c.acc[3][4], c.b[5]); \
  b_load<-1024>(c.b[5], vlane, B_STEP(9)); \
  grp<0, 11, 112>(c.acc[0][5], c.acc[1][5], c.acc[2][5], c.acc[3][5], c.b[6]); \
  b_load<0>(c.b[6], vlane, B_STEP(9)); \
  grp<0, 11, 112>(c.acc[0][6], c.acc[1][6], c.acc[2][6], c.acc[3][6], c.b[7]); \
  b_load<1024>(c.b[7], vlane, B_STEP(9)); \
  grp<0, 11, 128>(c.acc[0][0], c.acc[1][0], c.acc[2][0], c.acc[3][0], c.b[8]); \
  b_load<2048>(c.b[8], vlane, B_STEP(9)); \
  grp<0, 11, 128>(c.acc[0][1], c.acc[1][1], c.acc[2][1], c.acc[3][1], c.b[9]); \
  b_load<3072>(c.b[9], vlane, B_STEP(9)); \
  grp<0, 11, 128>(c.acc[0][2], c.acc[1][2], c.acc[2][2], c.acc[3][2], c.b[10]); \
  b_load<-3072>(c.b[10], vlane, B_STEP(10)); \
  grp<0, 11, 128>(c.acc[0][3], c.acc[1][3], c.acc[2][3], c.acc[3][3], c.b[11]); \
  b_load<-2048>(c.b[11], vlane, B_STEP(10)); \
  grp<0, 11, 128>(c.acc[0][4], c.acc[1][4], c.acc[2][4], c.acc[3][4], c.b[0]); \
  b_load<-1024>(c.b[0], vlane, B_STEP(10)); \
  grp<0, 11, 128>(c.acc[0][5], c.acc[1][5], c.acc[2][5], c.acc[3][5], c.b[1]); \
  b_load<0>(c.b[1], vlane, B_STEP(10)); \
  grp<0, 11, 128>(c.acc[0][6], c.acc[1][6], c.acc[2][6], c.acc[3][6], c.b[2]); \
  b_load<1024>(c.b[2], vlane, B_STEP(10)); \
  grp<0, 11, 144>(c.acc[0][0], c.acc[1][0], c.acc[2][0], c.acc[3][0], c.b[3]); \
  b_load<2048>(c.b[3], vlane, B_STEP(10)); \
  grp<0, 11, 144>(c.acc[0][1], c.acc[1][1], c.acc[2][1], c.acc[3][1], c.b[4]); \
  b_load<3072>(c.b[4], vlane, B_STEP(10)); \
  grp<0, 11, 144>(c.acc[0][2], c.acc[1][2], c.acc[2][2], c.acc[3][2], c.b[5]); \
  b_load<-3072>(c.b[5], vlane, B_STEP(11)); \
  grp<0, 11, 144>(c.acc[0][3], c.acc[1][3], c.acc[2][3], c.acc[3][3], c.b[6]); \
  b_load<-2048>(c.b[6], vlane, B_STEP(11)); \
  grp<0, 11, 144>(c.acc[0][4], c.acc[1][4], c.acc[2][4], c.acc[3][4], c.b[7]); \
  b_load<-1024>(c.b[7], vlane, B_STEP(11)); \
  grp<0, 11, 144>(c.acc[0][5], c.acc[1][5], c.acc[2][5], c.acc[3][5], c.b[8]); \
  b_load<0>(c.b[8], vlane, B_STEP(11)); \
  grp<0, 11, 144>(c.acc[0][6], c.acc[1][6], c.acc[2][6], c.acc[3][6], c.b[9]); \
  b_load<1024>(c.b[9], vlane, B_STEP(11)); \
  grp<0, 11, 160>(c.acc[0][0], c.acc[1][0], c.acc[2][0], c.acc[3][0], c.b[10]); \
  b_load<2048>(c.b[10], vlane, B_STEP(11)); \
  grp<0, 11, 160>(c.acc[0][1], c.acc[1][1], c.acc[2][1], c.acc[3][1], c.b[11]); \
  b_load<3072>(c.b[11], vlane, B_STEP(11)); \
  grp<0, 11, 160>(c.acc[0][2], c.acc[1][2], c.acc[2][2], c.acc[3][2], c.b[0]); \
  b_load<-3072>(c.b[0], vlane, B_STEP(12)); \
  grp<0, 11, 160>(c.acc[0][3], c.acc[1][3], c.acc[2][3], c.acc[3][3], c.b[1]); \
  b_load<-3072>(c.b[1], vlane, B_STEP(13)); \
  grp<0, 11, 160>(c.acc[0][4], c.acc[1][4], c.acc[2][4], c.acc[3][4], c.b[2]); \
  b_load<-3072>(c.b[2], vlane, B_STEP(14)); \
  grp<0, 11, 160>(c.acc[0][5], c.acc[1][5], c.acc[2][5], c.acc[3][5], c.b[3]); \
  b_load<-3072>(c.b[3], vlane, B_STEP(15)); \
  grp<0, 11, 160>(c.acc[0][6], c.acc[1][6], c.acc[2][6], c.acc[3][6], c.b[4]); \
  b_load<-2048>(c.b[4], vlane, B_STEP(12)); \
  grp<0, 11, 176>(c.acc[0][0], c.acc[1][0], c.acc[2][0], c.acc[3][0], c.b[5]); \
  b_load<-2048>(c.b[5], vlane, B_STEP(13)); \
  grp<0, 11, 176>(c.acc[0][1], c.acc[1][1], c.acc[2][1], c.acc[3][1], c.b[6]); \
  b_load<-2048>(c.b[6], vlane, B_STEP(14)); \
  grp<0, 11, 176>(c.acc[0][2], c.acc[1][2], c.acc[2][2], c.acc[3][2], c.b[7]); \
  b_load<-2048>(c.b[7], vlane, B_STEP(15)); \
  grp<0, 11, 176>(c.acc[0][3], c.acc[1][3], c.acc[2][3], c.acc[3][3], c.b[8]); \
  b_load<-1024>(c.b[8], vlane, B_STEP(12)); \
  barrier(); \
  grp<0, 11, 176>(c.acc[0][4], c.acc[1][4], c.acc[2][4], c.acc[3][4], c.b[9]); \
  b_load<-1024>(c.b[9], vlane, B_STEP(13)); \
  grp<0, 11, 176>(c.acc[0][5], c.acc[1][5], c.acc[2][5], c.acc[3][5], c.b[10]); \
  b_load<-1024>(c.b[10], vlane, B_STEP(14)); \
  grp<0, 11, 176>(c.acc[0][6], c.acc[1][6], c.acc[2][6], c.acc[3][6], c.b[11]); \
  b_load<-1024>(c.b[11], vlane, B_STEP(15)); \
  grp<0, 11, 192>(c.acc[0][0], c.acc[1][0], c.acc[2][0], c.acc[3][0], c.b[0]); \
  b_load<0>(c.b[0], vlane, B_STEP(12)); \
  grp<0, 11, 208>(c.acc[0][0], c.acc[1][0], c.acc[2][0], c.acc[3][0], c.b[1]); \
  b_load<0>(c.b[1], vlane, B_STEP(13)); \
  grp<0, 11, 224>(c.acc[0][0], c.acc[1][0], c.acc[2][0], c.acc[3][0], c.b[2]); \
  b_load<0>(c.b[2], vlane, B_STEP(14)); \
  grp<0, 11, 240>(c.acc[0][0], c.acc[1][0], c.acc[2][0], c.acc[3][0], c.b[3]); \
  b_load<0>(c.b[3], vlane, B_STEP(15)); \
  grp<0, 11, 192>(c.acc[0][1], c.acc[1][1], c.acc[2][1], c.acc[3][1], c.b[4]); \
  b_load<1024>(c.b[4], vlane, B_STEP(12)); \
  RED_WRITE2(0, 0); \
  grp<0, 11, 208>(c.acc[0][1], c.acc[1][1], c.acc[2][1], c.acc[3][1], c.b[5]); \
  b_load<1024>(c.b[5], vlane, B_STEP(13)); \
  RED_WRITE2(1, 0); \
  grp<0, 11, 224>(c.acc[0][1], c.acc[1][1], c.acc[2][1], c.acc[3][1], c.b[6]); \
  b_load<1024>(c.b[6], vlane, B_STEP(14)); \
  RED_WRITE2(2, 0); \
  grp<0, 11, 240>(c.acc[0][1], c.acc[1][1], c.acc[2][1], c.acc[3][1], c.b[7]); \
  b_load<1024>(c.b[7], vlane, B_STEP(15)); \
  RED_WRITE2(3, 0); \
  grp<0, 11, 192>(c.acc[0][2], c.acc[1][2], c.acc[2][2], c.acc[3][2], c.b[8]); \
  b_load<2048>(c.b[8], vlane, B_STEP(12)); \
  RED_WRITE2(0, 1); \
  grp<0, 11, 208>(c.acc[0][2], c.acc[1][2], c.acc[2][2], c.acc[3][2], c.b[9]); \
  b_load<2048>(c.b[9], vlane, B_STEP(13)); \
  RED_WRITE2(1, 1); \
  grp<0, 11, 224>(c.acc[0][2], c.acc[1][2], c.acc[2][2], c.acc[3][2], c.b[10]); \
  b_load<2048>(c.b[10], vlane, B_STEP(14)); \
  RED_WRITE2(2, 1); \
  grp<0, 11, 240>(c.acc[0][2], c.acc[1][2], c.acc[2][2], c.acc[3][2], c.b[11]); \
  b_load<2048>(c.b[11], vlane, B_STEP(15)); \
  RED_WRITE2(3, 1); \
  grp<0, 11, 192>(c.acc[0][3], c.acc[1][3], c.acc[2][3], c.acc[3][3], c.b[0]); \
  b_load<3072>(c.b[0], vlane, B_STEP(12)); \
  RED_WRITE2(0, 2); \
  grp<0, 11, 208>(c.acc[0][3], c.acc[1][3], c.acc[2][3], c.acc[3][3], c.b[1]); \
  b_load<3072>(c.b[1], vlane, B_STEP(13)); \
  RED_WRITE2(1, 2); \
  grp<0, 11, 224>(c.acc[0][3], c.acc[1][3], c.acc[2][3], c.acc[3][3], c.b[2]); \
  b_load<3072>(c.b[2], vlane, B_STEP(14)); \
  RED_WRITE2(2, 2); \
  grp<0, 11, 240>(c.acc[0][3], c.acc[1][3], c.acc[2][3], c.acc[3][3], c.b[3]); \
  b_load<3072>(c.b[3], vlane, B_STEP(15)); \
  RED_WRITE2(3, 2); \
  grp<0, 11, 192>(c.acc[0][4], c.acc[1][4], c.acc[2][4], c.acc[3][4], c.b[4]); \
  XR_LOAD(0); \
  XR_LOAD(1); \
  XR_LOAD(2); \
  XR_LOAD(3); \
  FFN_TOUCH_ISSUE(); \
  RED_WRITE2(0, 3); \
  grp<0, 15, 208>(c.acc[0][4], c.acc[1][4], c.acc[2][4], c.acc[3][4], c.b[5]); \
  RED_WRITE2(1, 3); \
  grp<0, 14, 224>(c.acc[0][4], c.acc[1][4], c.acc[2][4], c.acc[3][4], c.b[6]); \
  RED_WRITE2(2, 3); \
  grp<0, 13, 240>(c.acc[0][4], c.acc[1][4], c.acc[2][4], c.acc[3][4], c.b[7]); \
  RED_WRITE2(3, 3); \
  grp<0, 12, 192>(c.acc[0][5], c.acc[1][5], c.acc[2][5], c.acc[3][5], c.b[8]); \
  RED_WRITE2(0, 4); \
  grp<0, 11, 208>(c.acc[0][5], c.acc[1][5], c.acc[2][5], c.acc[3][5], c.b[9]); \
  RED_WRITE2(1, 4); \
  grp<0, 10, 224>(c.acc[0][5], c.acc[1][5], c.acc[2][5], c.acc[3][5], c.b[10]); \
  RED_WRITE2(2, 4); \
  grp<0, 9, 240>(c.acc[0][5], c.acc[1][5], c.acc[2][5], c.acc[3][5], c.b[11]); \
  RED_WRITE2(3, 4); \
  grp<0, 8, 192>(c.acc[0][6], c.acc[1][6], c.acc[2][6], c.acc[3][6], c.b[0]); \
  RED_WRITE2(0, 5); \
  grp<0, 7, 208>(c.acc[0][6], c.acc[1][6], c.acc[2][6], c.acc[3][6], c.b[1]); \
  RED_WRITE2(1, 5); \
  grp<0, 6, 224>(c.acc[0][6], c.acc[1][6], c.acc[2][6], c.acc[3][6], c.b[2]); \
  RED_WRITE2(2, 5); \
  grp<0, 5, 240>(c.acc[0][6], c.acc[1][6], c.acc[2][6], c.acc[3][6], c.b[3]); \
  RED_WRITE2(3, 5); \
  MFMA_SETTLE(); \
  RED_WRITE(6); \
} while (0)
#define GEN_RING 12


#define P1_ACC_PIN(NOPS) asm volatile(NOPS : "+v"(a.acc[0][0]), "+v"(a.acc[0][1]), "+v"(a.acc[0][2]), "+v"(a.acc[0][3]), "+v"(a.acc[1][0]), "+v"(a.acc[1][1]), \
    "+v"(a.acc[1][2]), "+v"(a.acc[1][3]), "+v"(a.aux[0]), "+v"(a.aux[1]), "+v"(a.aux[2]))
#define A_SETUP() do { \
    A.rsC = __builtin_amdgcn_make_buffer_rsrc((void*)cmat, 0, S * CROWB, 0x00020000); \
    A.v = (lane >> 3) * CROWB + (((lane & 7) ^ (4 * (W & 1) + (lane >> 4))) * 16); \
    A.ldsw = rfl(c.lds0 + (W >> 1) * 8192 + (W & 1) * 1024); \
    _Pragma("unroll") for (int r = 0; r < 8; ++r) A.src[r] = rfl(asrc + (W & 1) * 8 * CROWB + (W >> 1) * 1024 + ((r + arot) & 7) * 128); \
    A.r0 = c.lds0 + (W >> 1) * 8192 + (lane & 15) * 128 + (((lane >> 4) ^ (((lane & 15) >> 1) & 7)) * 16); A.r1 = A.r0 ^ 64; A.r0h = A.r0 + 65536; A.r1h = A.r1 + 65536; } while (0)
#define P1_INIT() do { \
    _Pragma("unroll") for (int u = 0; u < 2; ++u) _Pragma("unroll") for (int t = 0; t < 4; ++t) a.acc[u][t] = (f32x4){0.f, 0.f, 0.f, 0.f}; \
    _Pragma("unroll") for (int t = 0; t < 3; ++t) a.aux[t] = (f32x4){0.f, 0.f, 0.f, 0.f}; \
    asm volatile("" : "+v"(ones)); P1_ACC_PIN("s_nop 1"); } while (0)
#define XW_TOUCH() do { \
    unsigned l2 = lane, bb = blockIdx.x; asm volatile("" : "+v"(l2), "+s"(bb));        \
    const unsigned xg_ = bb & 7, j_ = bb >> 3, p_ = xg_ >> 2, m_ = (xg_ & 3) * 8 + (j_ >> 2), n_ = j_ & 3; \
      \
    constexpr unsigned CNT = W < 2 ? 49 : 56; \
    const char* share = W < 2 ? (const char*)(p_ ? ar.wim : ar.wre) + (size_t)(112 * n_ + (m_ & 7) * 14) * ROWB : (const char*)ar.x + (size_t)(64 * m_ + 16 * n_) * ROWB; \
    const char* line = share + ((W & 1) * CNT + min(l2, CNT - 1)) * 128; \
    if (W == 0 && l2 >= 49)        \
      line = code0 + (min(j_ * 11, (unsigned)CODE_LINES - 11) + min(l2 - 49, 10u)) * 128; \
    touch(xw_sink, line); \
    if (W == 0) {        \
      const unsigned gi_ = xg_ + 8 * j_, po = (j_ < 4 && gi_ < 28) ? (unsigned)WS_GU + gi_ * 1024 : bb - 64 < 7u ? (unsigned)WS_XS + (bb - 64) * 1024 : bb - 72 < 7u ? (unsigned)WS_HS + (bb - 72) * 1024 : 0u; \
      if (po) { const v4u ones128 = {~0u, ~0u, ~0u, ~0u}; st16_sc1(ar.ws, po + l2 * 16, ones128); } } \
    bfp = (gu32*)(ar.ws + WS_BF + (size_t)(((p_ * 4 + n_) * 64 + 2 * m_ + (W & 1)) * 7168 + (W >> 1) * 4096)); \
    flag1 = (gu32*)(ar.ws + WS_FLAG1) + ((p_ * 4 + n_) * 32 + m_) * 32; \
    cmat = p_ ? ar.cim : ar.cre; sgn = p_ ? -1.f : 1.f; \
    asrc = (((xg_ & 1) * 16 + (j_ >> 1)) * 64) * CROWB + ((xg_ >> 1) & 1) * 2048; arot = 2 * ((j_ >> 1) & 3); } while (0)
#define P1_FINISH_STATS() do { asm volatile("" :: "v"(xw_sink)); P1_ACC_PIN("s_nop 7\n\ts_nop 1"); p1_stats_out<W>(a, smem, lane); wait_lgkm<0>(); barrier(); } while (0)
#define P1_EPILOGUE_COMPUTE() p1_epilogue<W>(a, smem, lane, sgn, o)
#define P1_STORE(t) asm volatile("global_store_dwordx4 %0, %1, %2 offset:%c3 sc1\n\ts_nop 1" :: "v"(vl16), "v"(o[t]), "s"(bfp), "i"((t) * 1024) : "memory")
#define P1_FLAG() do { if (W == 0 && lane == 0) __hip_atomic_store(flag1, tag, RLX_AGENT); } while (0)

template <int W> DI void p1_stats_out(const P1Acc& a, char* smem, int lane) {
  constexpr int sp = W & 1;
  float* st_w = (float*)(smem + P1_STAT); float* st_s = st_w + 112; float* st_q = st_s + 64;
  const int i = lane & 15, q = lane >> 4, j = lane & 3;
  auto diag = [&](const f32x4& v) { return j == 0 ? v[0] : j == 1 ? v[1] : j == 2 ? v[2] : v[3]; };
  if constexpr (W == 0) { if (lane < 16) { st_w[lane] = a.aux[0][0]; st_w[16 + lane] = a.aux[1][0]; st_w[32 + lane] = a.aux[2][0]; } }
  if constexpr (W == 1) { if (lane < 16) st_w[48 + lane] = a.aux[0][0]; }
  if constexpr (W == 2) { if (lane < 16) st_w[64 + lane] = a.aux[0][0]; }
  if constexpr (W == 3) { if (lane < 16) { st_w[80 + lane] = a.aux[0][0]; st_w[96 + lane] = a.aux[1][0]; } }
  if constexpr (W == 1 || W == 2) { if ((i >> 2) == q) { st_q[32 * sp + 8 * q + j] = diag(a.aux[1]); st_q[32 * sp + 8 * q + 4 + j] = diag(a.aux[2]); } }
  if constexpr (W >= 2) { if (i == 0) { *(f32x4*)(st_s + 32 * sp + 8 * q) = a.acc[0][3]; *(f32x4*)(st_s + 32 * sp + 8 * q + 4) = a.acc[1][3]; } }
}
template <int W> DI void p1_epilogue(const P1Acc& a, const char* smem, int lane, float sgn, v4u (&o)[4]) {
  constexpr int sp = W & 1, nt0 = (W >> 1) * 4, NT = W < 2 ? 4 : 3;
  const float* st_w = (const float*)(smem + P1_STAT); const float* st_s = st_w + 112; const float* st_q = st_s + 64;
  const int i = lane & 15, q = lane >> 4;
  float ws[4];
  #pragma unroll
  for (int t = 0; t < NT; ++t) ws[t] = st_w[(nt0 + t) * 16 + i];
  float* pv = (float*)(smem + P1_STAT + 1024 + W * 256);
  {
    const float mean = st_s[32 * sp + (lane & 31)] * (1.f / D);
    const float r = sgn * inv_std(st_q[32 * sp + (lane & 31)] * (1.f / D) - mean * mean);
    pv[lane] = lane < 32 ? r : rbf(mean) * r;
  }
  float rs[2][4], mr[2][4];
  #pragma unroll
  for (int u = 0; u < 2; ++u) {
    const f32x4 a4 = *(const f32x4*)(pv + 8 * q + 4 * u), b4 = *(const f32x4*)(pv + 32 + 8 * q + 4 * u);
    #pragma unroll
    for (int j = 0; j < 4; ++j) { rs[u][j] = a4[j]; mr[u][j] = b4[j]; }
  }
  #pragma unroll
  for (int t = 0; t < NT; ++t) {
    float v[2][4];
    #pragma unroll
    for (int u = 0; u < 2; ++u)
      #pragma unroll
      for (int j = 0; j < 4; ++j) v[u][j] = a.acc[u][t][j] * rs[u][j] - mr[u][j] * ws[t];
    o[t] = (v4u){cvtpk(v[0][0], v[0][1]), cvtpk(v[0][2], v[0][3]), cvtpk(v[1][0], v[1][1]), cvtpk(v[1][2], v[1][3])};
  }
}
template <int W> DI void phase1(const P1Ctx& c, ACtx& A, const Args& ar, const char* code0, char* smem, int lane, unsigned tag) {
  P1Acc a; unsigned xw_sink;
  gu32 *bfp, *flag1; const u16* cmat; unsigned asrc, arot; float sgn;
  bf16x8 xa0[2], wb0[4], xa1[2], wb1[4];
  bf16x8 ones = {0x3f80, 0x3f80, 0x3f80, 0x3f80, 0x3f80, 0x3f80, 0x3f80, 0x3f80};
  const int i = lane & 15, q = lane >> 4;
  unsigned f0 = c.lds0 + i * 128 + ((q ^ ((i >> 1) & 7)) * 16), f1 = f0 ^ 64;
  const unsigned vl16 = lane * 16;
  v4u o[4];
  if constexpr (W == 0) GEN_P1_W0; else if constexpr (W == 1) GEN_P1_W1; else if constexpr (W == 2) GEN_P1_W2; else GEN_P1_W3;
}

struct Main { f32x4 acc[4][7]; bf16x8 b[GEN_RING]; };

template <int CTRL> DI unsigned dpp_u(unsigned v) { return (unsigned)__builtin_amdgcn_update_dpp(0, (int)v, CTRL, 0xf, 0xf, true); }
template <int CTRL> DI float dpp_f(float v) { return __uint_as_float(dpp_u<CTRL>(__float_as_uint(v))); }
DI float half_sum(float v) {
  v += dpp_f<0xB1>(v); v += dpp_f<0x4E>(v); v += dpp_f<0x141>(v); v += dpp_f<0x140>(v);
  auto a = __builtin_amdgcn_permlane16_swap(__float_as_uint(v), __float_as_uint(v), false, false); return __uint_as_float(a[0]) + __uint_as_float(a[1]);
}
DI void half_sum3(float& a, float& b, float& c) {
  #define RS_STEP(CTRL) "v_add_f32_dpp %0, %0, %0 " CTRL " row_mask:0xf bank_mask:0xf\n\tv_add_f32_dpp %1, %1, %1 " CTRL " row_mask:0xf bank_mask:0xf\n\t" \
                        "v_add_f32_dpp %2, %2, %2 " CTRL " row_mask:0xf bank_mask:0xf\n\t"
  asm volatile("s_nop 1\n\t" RS_STEP("quad_perm:[1,0,3,2]") RS_STEP("quad_perm:[2,3,0,1]") RS_STEP("row_half_mirror") RS_STEP("row_mirror") : "+v"(a), "+v"(b), "+v"(c));
  #undef RS_STEP
  auto sw16 = [](float x) { auto r = __builtin_amdgcn_permlane16_swap(__float_as_uint(x), __float_as_uint(x), false, false); return __uint_as_float(r[0]) + __uint_as_float(r[1]); };
  a = sw16(a); b = sw16(b); c = sw16(c);
}
DI unsigned no_poison(unsigned v) { unsigned r; asm("v_pk_min_u16 %0, %1, %2" : "=v"(r) : "v"(v), "v"(0xfffefffeu)); return r; }
DI unsigned order16(unsigned r16) {
  const unsigned o = (r16 & 0x8000u) ? (~r16 & 0xffffu) : (r16 | 0x8000u);
  return (r16 & 0x7fffu) > 0x7f80u ? 0u : o;
}
DI void bld16(v4u& d, __amdgpu_buffer_rsrc_t rs, unsigned voff, unsigned soff) { asm volatile("buffer_load_dwordx4 %0, %1, %2, %3 offen" : "=v"(d) : "v"(voff), "s"(rs), "s"(soff) : "memory"); }
DI void bld16_sc1(v4u& d, __amdgpu_buffer_rsrc_t rs, unsigned voff, unsigned soff) { asm volatile("buffer_load_dwordx4 %0, %1, %2, %3 offen sc1" : "=v"(d) : "v"(voff), "s"(rs), "s"(soff) : "memory"); }
template <int OFF> DI void lds_rd(f32x4& d, unsigned addr) { asm volatile("ds_read_b128 %0, %1 offset:%c2" : "=v"(d) : "v"(addr), "i"(OFF) : "memory"); }
DI f32x2 unpk(unsigned v) { return (f32x2){__uint_as_float(v << 16), __uint_as_float(v & 0xffff0000u)}; }
DI void st8_sc1(void* base, unsigned off, const v2u& v) { asm volatile("global_store_dwordx2 %0, %1, %2 sc1\n\ts_nop 1" :: "v"(off), "v"(v), "s"(base) : "memory"); }
template <int OFF> DI void tr_read(v2u& dst, unsigned addr) { asm volatile("ds_read_b64_tr_b16 %0, %1 offset:%c2" : "=v"(dst) : "v"(addr), "i"(OFF) : "memory"); }
DI bf16x8 frag(const v2u& a, const v2u& b) { return __builtin_bit_cast(bf16x8, (v4u){a.x, a.y, b.x, b.y}); }
DI bf16x8 frag(const v4u& a) { return __builtin_bit_cast(bf16x8, a); }


__global__ __launch_bounds__(256, 1) void fnet_moe_kernel(Args a) {
  extern __shared__ __attribute__((aligned(16))) char smem[];
  const int tid = threadIdx.x, lane = tid & 63;
  const unsigned w = rfl(tid >> 6);
  const int b = blockIdx.x, xg = b & 7, j = b >> 3, p = xg >> 2;
  const int m = (xg & 3) * 8 + (j >> 2), n = j & 3;
  const int kh = (xg >> 1) & 1, mt2 = (xg & 1) * 16 + (j >> 1), nh = j & 1;
  const unsigned kq = w >> 1, n2 = 2 * nh + (w & 1), kq4 = 2 * kh + kq, ph = (j >> 1) & 3;
  asm volatile("" :: "s"(a.x), "s"(a.cre), "s"(a.cim), "s"(a.wre), "s"(a.wim), "s"(a.wr), "s"(a.wg), "s"(a.wu), "s"(a.wd), "s"(a.out), "s"(a.ws));
  const char* code0;
  asm volatile("s_getpc_b64 %0" : "=s"(code0));
  const unsigned tag = *(const __attribute__((address_space(4))) unsigned*)(a.ws + WS_EPOCH) + 1u;
  const unsigned lds0 = rfl((unsigned)(size_t)smem);

  const bool isG = j < 4 && xg + 8 * j < 28, isD = j >= 4 && j < 8 && xg < 7;
  const int gi = xg + 8 * j, di = 4 * xg + (j - 4);
  ACtx A;
  {
    P1Ctx c;
    c.rsX = __builtin_amdgcn_make_buffer_rsrc((void*)a.x, 0, S * ROWB, 0x00020000);
    c.rsW = __builtin_amdgcn_make_buffer_rsrc((void*)(p ? a.wim : a.wre), 0, D * ROWB, 0x00020000);
    const unsigned par = w & 1, sw16 = (((lane & 7) ^ (4 * par + (lane >> 4))) * 16);
    c.vx = (8 * (2 * par + (lane >> 5)) + ((lane >> 3) & 3)) * ROWB + sw16;
    c.vw = (lane >> 3) * ROWB + sw16;
    c.sx = rfl((unsigned)(m * 64 * ROWB)); c.sw = rfl((unsigned)(n * 112 * ROWB)); c.lds0 = lds0;
    if (w == 0) phase1<0>(c, A, a, code0, smem, lane, tag); else if (w == 1) phase1<1>(c, A, a, code0, smem, lane, tag);
    else if (w == 2) phase1<2>(c, A, a, code0, smem, lane, tag); else phase1<3>(c, A, a, code0, smem, lane, tag);
  }
  A.rsC = __builtin_amdgcn_make_buffer_rsrc((void*)(p ? a.cim : a.cre), 0, S * CROWB, 0x00020000);
  A.ldsw = rfl(A.ldsw);
  #pragma unroll
  for (int r = 0; r < 8; ++r) A.src[r] = rfl(A.src[r]);
  const int own = p * 4 + kh * 2 + nh, t0 = mt2 * 64 + own * 8 + (int)w * 2;
  const int hl = lane >> 5, c28 = lane & 31; const bool act = c28 < 28;
  const int n4 = lane >> 4, cc = lane & 15;
  constexpr unsigned OOB = 0xf0000000u;
  const unsigned rowv = act ? hl * ROWB + c28 * 16 : OOB, wrvo = act ? c28 * 16 : OOB;
  const __amdgpu_buffer_rsrc_t rsX2 = __builtin_amdgcn_make_buffer_rsrc((void*)a.x, 0, S * ROWB, 0x00020000), rsWr = __builtin_amdgcn_make_buffer_rsrc((void*)a.wr, 0, ROWB, 0x00020000);
  const unsigned sx0 = rfl((unsigned)(t0 * ROWB));
  v4u xr0, xr1, wr0, wr1;
  unsigned ffn_sink;
  const char* ffn_line;
  {
    const unsigned ng = xg < 4 ? 4 : 3, idx = j * 168 + w * 42 + (lane < 42 ? lane : 41);
    if (idx < ng * 896) { const unsigned gq = idx / 896, rr = idx - gq * 896, row = rr < 448 ? rr : rr - 448;
      ffn_line = (const char*)(rr < 448 ? a.wg : a.wu) + (size_t)row * (F * 2) + (xg + 8 * gq) * 128; }
    else { const unsigned f = min(idx - ng * 896, 1791u); ffn_line = (const char*)a.wd + (size_t)f * ROWB + min(xg, 6) * 128; }
    if (w == 0 && lane >= 42) ffn_line = code0 + (size_t)(CODE_LINES - TAIL_LINES + min((unsigned)(j * (TAIL_LINES / 32)) + min((unsigned)(lane - 42), (unsigned)(TAIL_LINES / 32)), (unsigned)TAIL_LINES - 1)) * 128;
  }
#define FFN_TOUCH_ISSUE() touch(ffn_sink, ffn_line)
  Main c;
  {
    const unsigned vlane = lane * 16;
    const char* gb = a.ws + WS_BF + (size_t)(((p * 4 + n2) * 64 + kq4 * 16) * 7168) + 3072;
    const gu32* fl = (const gu32*)(a.ws + WS_FLAG1) + ((p * 4 + n2) * 32 + kq4 * 8 + (lane & 7)) * 32;
    const unsigned redw = lds0 + kq * RED_SRC + (lane & 15) * RED_RS + ((lane >> 4) & 1) * 448 + ((w & 1) * 14 + (lane >> 5)) * 16;
#define B_STEP(kk) (gb + (((kk) + 4 * ph) & 15) * 7168)
    unsigned fv;
#define POLL_ISSUE() asm volatile("global_load_dword %0, %1, off sc1" : "=v"(fv) : "v"(fl) : "memory")
#define POLL_CHECK() do { asm volatile("" : "+v"(fv)); \
      if (!__all(fv == tag)) for (unsigned spins = 0; spins < SPIN_CAP; ++spins) { __builtin_amdgcn_s_sleep(1); \
        asm volatile("global_load_dword %0, %1, off sc1\n\ts_waitcnt vmcnt(0)" : "=&v"(fv) : "v"(fl) : "memory"); if (__all(fv == tag)) break; } } while (0)
#define MFMA_SETTLE() asm volatile("s_nop 7\n\ts_nop 1" ::: "memory")
#define RED_WRITE(nt) do { lds_write128<(nt) * 32>(redw, c.acc[0][nt]); lds_write128<16 * RED_RS + (nt) * 32>(redw, c.acc[1][nt]); \
      lds_write128<32 * RED_RS + (nt) * 32>(redw, c.acc[2][nt]); lds_write128<48 * RED_RS + (nt) * 32>(redw, c.acc[3][nt]); } while (0)
#define RED_WRITE2(mt, nt) lds_write128<(mt) * 16 * RED_RS + (nt) * 32>(redw, c.acc[mt][nt])
#define XR_LOAD(k) do { if ((k) == 0) bld16(xr0, rsX2, rowv, sx0); else if ((k) == 1) bld16(xr1, rsX2, rowv, sx0 + 448); else if ((k) == 2) bld16(wr0, rsWr, wrvo, 0); else bld16(wr1, rsWr, wrvo, 448); } while (0)
    GEN_COMMON;
  }
  wait_lgkm<0>();
  barrier();
  const __amdgpu_buffer_rsrc_t rsY = __builtin_amdgcn_make_buffer_rsrc((void*)(a.ws + WS_Y), 0, Y_BYTES, 0x00020000);
  {
    const unsigned rb = 16 * w + hl;
    const unsigned la = lds0 + rb * RED_RS + c28 * 16, lb = la + RED_SRC;
    const unsigned so = act ? ((own * S + mt2 * 64 + rb) * 448 + c28 * 16) : OOB;
    f32x4 q[3][4];
    #define E_RD(it) do { lds_rd<(it) * 2 * RED_RS>(q[(it) % 3][0], la); lds_rd<(it) * 2 * RED_RS>(q[(it) % 3][1], lb); \
                          lds_rd<(it) * 2 * RED_RS + 448>(q[(it) % 3][2], la); lds_rd<(it) * 2 * RED_RS + 448>(q[(it) % 3][3], lb); } while (0)
    #define E_ST(it, n) do { asm volatile("s_waitcnt lgkmcnt(" #n ")" : "+v"(q[(it) % 3][0]), "+v"(q[(it) % 3][1]), "+v"(q[(it) % 3][2]), "+v"(q[(it) % 3][3]) :: "memory"); \
      const f32x4 lo = q[(it) % 3][0] + q[(it) % 3][1], hi = q[(it) % 3][2] + q[(it) % 3][3]; \
      const v4u o = {cvtpk(lo[0], lo[1]), cvtpk(lo[2], lo[3]), cvtpk(hi[0], hi[1]), cvtpk(hi[2], hi[3])}; \
      __builtin_amdgcn_raw_buffer_store_b128(o, rsY, so + ((it) & 3) * 896, ((it) >> 2) * 3584, 16); } while (0)
    E_RD(0); E_RD(1); E_RD(2);
    E_ST(0, 8); E_RD(3); E_ST(1, 8); E_RD(4); E_ST(2, 8); E_RD(5); E_ST(3, 8); E_RD(6); E_ST(4, 8); E_RD(7); E_ST(5, 8); E_ST(6, 4); E_ST(7, 0);
    #undef E_RD
    #undef E_ST
  }
  wait_vm<0>();
  asm volatile("" :: "v"(ffn_sink));
  barrier();
  if (tid == 0) __hip_atomic_store((gu32*)(a.ws + WS_FLAG2) + (mt2 * 8 + own) * 32, tag, RLX_AGENT);
  asm volatile("" : "+v"(xr0), "+v"(xr1), "+v"(wr0), "+v"(wr1));
  f32x2 wf[8]; float wsum;
  { f32x2 ws2 = {0.f, 0.f};
    #pragma unroll
    for (int k = 0; k < 4; ++k) { wf[k] = unpk(wr0[k]); wf[4 + k] = unpk(wr1[k]); ws2 += wf[k] + wf[4 + k]; }
    wsum = half_sum(ws2[0] + ws2[1]); }
  if (w == 0) {
    const gu32* fl = (const gu32*)(a.ws + WS_FLAG2) + (mt2 * 8 + (lane & 7)) * 32;
    for (unsigned spins = 0; spins < SPIN_CAP; ++spins) { if (__all(__hip_atomic_load(fl, RLX_AGENT) == tag)) break; __builtin_amdgcn_s_sleep(1); }
  }
  barrier();
  auto rows_load = [&](unsigned yv, v4u (&y)[8]) {
    bld16_sc1(y[0], rsY, yv, 0 * S * 448); bld16_sc1(y[1], rsY, yv, 1 * S * 448); bld16_sc1(y[2], rsY, yv, 2 * S * 448); bld16_sc1(y[3], rsY, yv, 3 * S * 448);
    bld16_sc1(y[4], rsY, yv, 4 * S * 448); bld16_sc1(y[5], rsY, yv, 5 * S * 448); bld16_sc1(y[6], rsY, yv, 6 * S * 448); bld16_sc1(y[7], rsY, yv, 7 * S * 448);
  };
  #define ROWS_LANDED(y, xa, xb) asm volatile("s_waitcnt vmcnt(0)" : "+v"(y[0]), "+v"(y[1]), "+v"(y[2]), "+v"(y[3]), "+v"(y[4]), "+v"(y[5]), "+v"(y[6]), "+v"(y[7]), "+v"(xa), "+v"(xb) :: "memory")
  bf16x8 sel1, sel2;
  { const unsigned one = (cc >> 2) == n4 ? ((cc & 1) ? 0x3f800000u : 0x00003f80u) : 0u;
    const unsigned e0 = (cc & 2) ? 0u : one, e1 = (cc & 2) ? one : 0u;
    sel1 = frag((v4u){e0, e1, 0u, 0u}); sel2 = frag((v4u){0u, 0u, e0, e1});
    asm volatile("" : "+v"(sel1), "+v"(sel2)); }
  auto row_sum = [&](const v4u (&y)[8], const v4u& xa, const v4u& xb, f32x2 (&h)[8]) {
    f32x4 a0, a1, b0, b1;
    #define SEL0(d, s_, v) asm volatile("v_mfma_f32_16x16x32_bf16 %0, %1, %2, 0" : "=&v"(d) : "v"(s_), "v"(v))
    #define SELA(d, s_, v) asm volatile("v_mfma_f32_16x16x32_bf16 %0, %1, %2, %0" : "+v"(d) : "v"(s_), "v"(v))
    SEL0(a0, sel1, xa); SEL0(a1, sel2, xa); SEL0(b0, sel1, xb); SEL0(b1, sel2, xb);
    #pragma unroll
    for (int k = 0; k < 8; k += 2) { SELA(a0, sel1, y[k]); SELA(a1, sel2, y[k]); SELA(b0, sel1, y[k + 1]); SELA(b1, sel2, y[k + 1]); }
    #undef SEL0
    #undef SELA
    asm volatile("s_nop 7\n\ts_nop 1" : "+v"(a0), "+v"(a1), "+v"(b0), "+v"(b1));
    h[0] = (f32x2){a0[0], a0[1]}; h[1] = (f32x2){a0[2], a0[3]}; h[2] = (f32x2){a1[0], a1[1]}; h[3] = (f32x2){a1[2], a1[3]};
    h[4] = (f32x2){b0[0], b0[1]}; h[5] = (f32x2){b0[2], b0[3]}; h[6] = (f32x2){b1[0], b1[1]}; h[7] = (f32x2){b1[2], b1[3]};
  };
  auto row_hat = [&](const f32x2 (&h)[8], float mean, float rstd, v4u (&np)[2]) {
    const float nm = -mean * rstd;
    #pragma unroll
    for (int k = 0; k < 8; ++k) { const f32x2 nn = h[k] * rstd + nm; np[k >> 2][k & 3] = cvtpk(nn[0], nn[1]); }
  };
  auto row_norm = [&](const f32x2 (&h)[8], float mean, float rstd, v4u (&hp)[2], v4u (&np)[2]) {
    #pragma unroll
    for (int k = 0; k < 8; ++k) {
      const unsigned q = cvtpk(h[k][0], h[k][1]);
      const f32x2 nn = (unpk(q) - mean) * rstd;
      hp[k >> 2][k & 3] = q; np[k >> 2][k & 3] = cvtpk(nn[0], nn[1]);
    }
  };
  f32x2 h[8]; float mean, rstd;
  {
    v4u y[8];
    rows_load(act ? (t0 + hl) * 448 + c28 * 16 : OOB, y);
    ROWS_LANDED(y, xr0, xr1);
    row_sum(y, xr0, xr1, h);
    f32x2 a1 = h[0], a2 = h[0] * h[0], a3 = h[0] * wf[0];
    #pragma unroll
    for (int k = 1; k < 8; ++k) { a1 += h[k]; a2 += h[k] * h[k]; a3 += h[k] * wf[k]; }
    float s1 = a1[0] + a1[1], s2 = a2[0] + a2[1], s3 = a3[0] + a3[1];
    half_sum3(s1, s2, s3);
    mean = s1 * (1.f / D);
    rstd = inv_std(s2 * (1.f / D) - mean * mean);
    mean = rbf(mean);
    const unsigned o16 = order16(cvtpk(rstd * (s3 - mean * wsum), 0.f) & 0xffffu);
    const unsigned ob = __builtin_amdgcn_readlane(o16, 32);
    if (lane == 0) st8_sc1(a.ws + WS_RG, (t0 >> 1) * 8, (v2u){o16 | (ob << 16), tag});
    if (c28 == 0) st16_sc1(a.ws + WS_ST, (t0 + hl) * 16, (v4u){__float_as_uint(mean), tag, __float_as_uint(rstd), tag});
  }
  if (isG) {
    const __amdgpu_buffer_rsrc_t rg = __builtin_amdgcn_make_buffer_rsrc((void*)a.wg, 0, D * F * 2, 0x00020000), ru = __builtin_amdgcn_make_buffer_rsrc((void*)a.wu, 0, D * F * 2, 0x00020000);
    const unsigned vo0 = (lane >> 3) * (F * 2) + (((lane & 7) ^ (((lane >> 4) & 1) << 1)) * 16), vo1 = vo0 ^ 64;
    #pragma unroll 1
    for (int i = 0; i < 14; i += 2) {
      const unsigned pc = w * 14 + i, so = rfl(pc * 8 * (F * 2) + gi * 128), lo = rfl(lds0 + pc * 1024);
      dma_piece<0, 0, 0>(rg, vo0, lo, so); dma_piece<57344, 0, 0>(ru, vo0, lo, so);
      dma_piece<1024, 8 * F * 2, 0>(rg, vo1, lo, so); dma_piece<57344 + 1024, 8 * F * 2, 0>(ru, vo1, lo, so);
    }
  } else if (isD) {
    const __amdgpu_buffer_rsrc_t rd = __builtin_amdgcn_make_buffer_rsrc((void*)a.wd, 0, F * ROWB, 0x00020000);
    const unsigned q16 = lane ^ (((lane >> 4) & 1) << 3), vo = (q16 >> 1) * ROWB + (q16 & 1) * 16;
    #pragma unroll 1
    for (int i = 0; i < 14; ++i) {
      const unsigned pc = w * 14 + i, so = rfl(pc * 32 * ROWB + di * 32), lo = rfl(lds0 + pc * 1024);
      dma_piece<0, 0, 0>(rd, vo, lo, so);
    }
  }
  v2u wa[28][2];
  const unsigned g4 = lane >> 4, q4 = (lane & 15) >> 2, p4 = lane & 3;
  unsigned selkey = 0;
  if (isG) {
    const unsigned xsl = lds0 + 147456;
    const unsigned fo0 = ((c28 >> 2) >> 1) * 1024 + (2 * w + hl + 8 * ((c28 >> 2) & 1) + 16 * (c28 & 3)) * 16;
    const unsigned fo1 = ((7 + (c28 >> 2)) >> 1) * 1024 + (2 * w + hl + 8 * ((7 + (c28 >> 2)) & 1) + 16 * (c28 & 3)) * 16;
    unsigned xw0 = act ? xsl + fo0 : lds0 + 146432 + lane * 16, xw1 = act ? xsl + fo1 : lds0 + 146432 + lane * 16;
    asm volatile("" : "+v"(xw0), "+v"(xw1));
    unsigned key[8];
    const char* rg = a.ws + WS_RG;
    const unsigned l8 = lane >> 3;
    unsigned ix[8];
    #pragma unroll
    for (int k = 0; k < 8; ++k) { ix[k] = 2047u - tid * 8 - k; asm volatile("" : "+v"(ix[k])); }
    const unsigned gw = lds0 + 131072 + l8 * 16 + w * 4, gr = lds0 + 131072 + l8 * 16;
    const unsigned candb = lds0 + 131072 + 1024 + w * 2080, scr = candb + 2064;
    const unsigned mys = lds0 + 131072 + 1024 + (lane >> 4) * 2080 + (lane & 15) * 4;
    for (unsigned spins = 0;; ++spins) {
      v4u g0, g1;
      asm volatile("global_load_dwordx4 %0, %2, %3 sc1\n\tglobal_load_dwordx4 %1, %2, %3 offset:16 sc1\n\ts_waitcnt vmcnt(0)" : "=&v"(g0), "=&v"(g1) : "v"(tid * 32), "s"(rg) : "memory");
      key[0] = (g0.x << 16) | ix[0]; key[1] = (g0.x & 0xffff0000u) | ix[1]; key[2] = (g0.z << 16) | ix[2]; key[3] = (g0.z & 0xffff0000u) | ix[3];
      key[4] = (g1.x << 16) | ix[4]; key[5] = (g1.x & 0xffff0000u) | ix[5]; key[6] = (g1.z << 16) | ix[6]; key[7] = (g1.z & 0xffff0000u) | ix[7];
      if (__builtin_amdgcn_ballot_w64((((g0.y ^ tag) | (g0.w ^ tag)) | ((g1.y ^ tag) | (g1.w ^ tag))) != 0) == 0 || spins >= SPIN_CAP) break;
      __builtin_amdgcn_s_sleep(1);
    }
    unsigned thr;
    {
      unsigned mx = max(max(max(key[0], key[1]), key[2]), max(max(max(key[3], key[4]), key[5]), max(key[6], key[7])));
      v4u gm;
      asm volatile("s_nop 1\n\tv_max_u32_dpp %0, %0, %0 quad_perm:[1,0,3,2] row_mask:0xf bank_mask:0xf\n\ts_nop 1\n\t"
                   "v_max_u32_dpp %0, %0, %0 quad_perm:[2,3,0,1] row_mask:0xf bank_mask:0xf\n\ts_nop 1\n\t"
                   "v_max_u32_dpp %0, %0, %0 row_half_mirror row_mask:0xf bank_mask:0xf\n\t"
                   "ds_write_b32 %2, %0\n\ts_waitcnt lgkmcnt(0)\n\ts_barrier\n\tds_read_b128 %1, %3\n\ts_waitcnt lgkmcnt(0)"
                   : "+v"(mx), "=&v"(gm) : "v"(gw), "v"(gr) : "memory");
      thr = max(max(gm.x, gm.y), max(gm.z, gm.w));
      thr = min(thr, dpp_u<0x128>(thr));
      auto a16 = __builtin_amdgcn_permlane16_swap(thr, thr, false, false); thr = min(a16[0], a16[1]);
      auto a32 = __builtin_amdgcn_permlane32_swap(thr, thr, false, false); thr = min(a32[0], a32[1]);
    }
    unsigned tail = candb;
    #pragma unroll
    for (int k = 0; k < 8; k += 2) {
      unsigned ta_, tb_, na, nb;
      asm volatile("v_cmp_ge_u32 vcc, %5, %7\n\tv_cmp_ge_u32 s[38:39], %6, %7\n\ts_bcnt1_i32_b64 %2, vcc\n\t"
                   "v_mbcnt_lo_u32_b32 %0, vcc_lo, 0\n\ts_bcnt1_i32_b64 %3, s[38:39]\n\tv_mbcnt_lo_u32_b32 %1, s38, 0\n\t"
                   "v_mbcnt_hi_u32_b32 %0, vcc_hi, %0\n\tv_mbcnt_hi_u32_b32 %1, s39, %1\n\t"
                   "v_lshl_add_u32 %0, %0, 2, %4\n\tv_lshl_add_u32 %4, %2, 2, %4\n\tv_lshl_add_u32 %1, %1, 2, %4\n\tv_lshl_add_u32 %4, %3, 2, %4\n\t"
                   "v_cndmask_b32 %0, %8, %0, vcc\n\tv_cndmask_b32 %1, %8, %1, s[38:39]\n\t"
                   "ds_write_b32 %0, %5\n\tds_write_b32 %1, %6"
                   : "=&v"(ta_), "=&v"(tb_), "=&s"(na), "=&s"(nb), "+v"(tail)
                   : "v"(key[k]), "v"(key[k + 1]), "v"(thr), "v"(scr) : "memory", "scc", "vcc", "s38", "s39");
    }
    const unsigned cnt = (tail - candb) >> 2;
    v4u cn; unsigned ki;
    asm volatile("ds_write_b32 %2, %3\n\tds_write_b32 %4, %5\n\ts_waitcnt lgkmcnt(0)\n\ts_barrier\n\tds_read_b128 %0, %6\n\tds_read_b32 %1, %7\n\ts_waitcnt lgkmcnt(0)"
                 : "=&v"(cn), "=&v"(ki) : "v"(lane < 4 ? tail + lane * 4 : scr), "v"(0u), "v"(lds0 + 131072 + 128 + w * 4), "v"(cnt), "v"(lds0 + 131072 + 128), "v"(mys) : "memory");
    if (b == 0 && tid == 0) __hip_atomic_store((gu32*)(a.ws + WS_EPOCH), tag, RLX_AGENT);
    const unsigned n0 = rfl(cn.x), n1 = rfl(cn.y), n2 = rfl(cn.z), n3 = rfl(cn.w);
    unsigned rank = 0;
    if (max(max(n0, n1), max(n2, n3)) <= 16) {
      const v4u* lists = (const v4u*)(smem + 131072 + 1024);
      #define RANK4(v) asm volatile("v_cmp_gt_u32 vcc, %1, %5\n\tv_cmp_gt_u32 s[38:39], %2, %5\n\tv_cmp_gt_u32 s[40:41], %3, %5\n\tv_cmp_gt_u32 s[42:43], %4, %5\n\t" \
          "v_addc_co_u32 %0, s[44:45], 0, %0, vcc\n\tv_addc_co_u32 %0, s[44:45], 0, %0, s[38:39]\n\tv_addc_co_u32 %0, s[44:45], 0, %0, s[40:41]\n\tv_addc_co_u32 %0, s[44:45], 0, %0, s[42:43]" \
          : "+v"(rank) : "v"(v.x), "v"(v.y), "v"(v.z), "v"(v.w), "v"(ki) : "vcc", "s38", "s39", "s40", "s41", "s42", "s43", "s44", "s45")
      const v4u a0 = lists[0], a1 = lists[1], b0 = lists[130], b1 = lists[131], c0 = lists[260], c1 = lists[261], d0 = lists[390], d1 = lists[391];
      RANK4(a0); RANK4(b0); RANK4(c0); RANK4(d0);
      if (n0 > 4) RANK4(a1);
      if (n1 > 4) RANK4(b1);
      if (n2 > 4) RANK4(c1);
      if (n3 > 4) RANK4(d1);
      if (max(max(n0, n1), max(n2, n3)) > 8) {
        #pragma unroll 1
        for (unsigned q = 2; q < 4; ++q) {
          const v4u va = lists[q], vb = lists[130 + q], vc = lists[260 + q], vd = lists[390 + q];
          if (q * 4 < n0) RANK4(va);
          if (q * 4 < n1) RANK4(vb);
          if (q * 4 < n2) RANK4(vc);
          if (q * 4 < n3) RANK4(vd);
        }
      }
      #undef RANK4
      const unsigned myn = lane < 16 ? n0 : lane < 32 ? n1 : lane < 48 ? n2 : n3;
      if ((unsigned)(lane & 15) >= myn) rank = 8;
    } else {
      unsigned bound = 0xffffffffu; bool first = true;
      rank = lane < 8 ? lane : 8;
      #pragma unroll 1
      for (int k = 0; k < 8; ++k) {
        unsigned best = 0;
        for (unsigned sgm = 0; sgm < 4; ++sgm) {
          const unsigned nn = sgm == 0 ? n0 : sgm == 1 ? n1 : sgm == 2 ? n2 : n3;
          const unsigned* lst = (const unsigned*)(smem + 131072 + 1024 + sgm * 2080);
          for (unsigned q = lane; q < nn; q += 64) { const unsigned v = lst[q]; if ((first || v < bound) && v >= best) best = v; }
        }
        for (int o = 32; o; o >>= 1) best = max(best, (unsigned)__shfl_xor(best, o));
        bound = rfl(best); first = false;
        if (lane == k) ki = bound;
      }
    }
    auto key_of = [&](unsigned rk) { return (unsigned)__builtin_amdgcn_readlane(ki, __builtin_ctzll(__builtin_amdgcn_ballot_w64(rank == rk))); };
    const unsigned k0 = key_of(2 * w), k1 = key_of(2 * w + 1);
    const unsigned ta = 2047u - (k0 & 0xffffu), tb = 2047u - (k1 & 0xffffu);
    if (gi == 0 && lane < 2) {
      const unsigned kx = lane ? k1 : k0, o16 = kx >> 16, rbits = (o16 & 0x8000u) ? (o16 & 0x7fffu) : (~o16 & 0xffffu);
      st8_sc1(a.ws + WS_SEL, (2 * w + lane) * 128, (v2u){(2047u - (kx & 0xffffu)) | (rbits << 16), tag});
    }
    v4u xa, xb, y[8], stv;
    const unsigned tr = hl ? tb : ta;
    { const unsigned xv = act ? tr * ROWB + c28 * 16 : OOB; bld16(xa, rsX2, xv, 0); bld16(xb, rsX2, xv, 448); }
    rows_load(act ? tr * 448 + c28 * 16 : OOB, y);
    asm volatile("global_load_dwordx4 %0, %1, %2 sc1" : "=v"(stv) : "v"(tr * 16), "s"(a.ws + WS_ST) : "memory");
    {
      const unsigned sw = ((q4 >> 1) & 1) | ((g4 & 1) << 1);
      const unsigned ad = lds0 + (8 * g4 + q4) * 128 + ((w ^ sw) * 32) + p4 * 8, ad2 = ad + 57344;
      #define G_RD(kk) tr_read<(kk) * 4096>(wa[kk][0], ad); tr_read<(kk) * 4096 + 512>(wa[kk][1], ad); tr_read<(kk) * 4096>(wa[14 + kk][0], ad2); tr_read<(kk) * 4096 + 512>(wa[14 + kk][1], ad2);
      G_RD(0) G_RD(1) G_RD(2) G_RD(3) G_RD(4) G_RD(5) G_RD(6) G_RD(7) G_RD(8) G_RD(9) G_RD(10) G_RD(11) G_RD(12) G_RD(13)
      #undef G_RD
      wait_lgkm<0>();
      #pragma unroll
      for (int k = 0; k < 28; ++k) asm volatile("" : "+v"(wa[k][0]), "+v"(wa[k][1]));
    }
    ROWS_LANDED(y, xa, xb);
    asm volatile("" : "+v"(stv));
    if (__builtin_amdgcn_ballot_w64(((stv.y ^ tag) | (stv.w ^ tag)) != 0) != 0)
      for (unsigned spins = 0; spins < SPIN_CAP; ++spins) {
        asm volatile("global_load_dwordx4 %0, %1, %2 sc1\n\ts_waitcnt vmcnt(0)" : "=&v"(stv) : "v"(tr * 16), "s"(a.ws + WS_ST) : "memory");
        if (__builtin_amdgcn_ballot_w64(((stv.y ^ tag) | (stv.w ^ tag)) != 0) == 0) break;
      }
    {
      f32x2 hs[8]; v4u np[2];
      row_sum(y, xa, xb, hs);
      row_hat(hs, __uint_as_float(stv.x), __uint_as_float(stv.z), np);
      asm volatile("ds_write_b128 %0, %1\n\tds_write_b128 %2, %3" :: "v"(xw0), "v"(np[0]), "v"(xw1), "v"(np[1]) : "memory");
      if (gi < 8 && (int)w == (gi >> 1) && hl == (gi & 1) && act) {
        auto np4 = [](const v4u& v) { return (v4u){no_poison(v[0]), no_poison(v[1]), no_poison(v[2]), no_poison(v[3])}; };
        v4u hp[2];
        #pragma unroll
        for (int k = 0; k < 8; ++k) hp[k >> 2][k & 3] = cvtpk(hs[k][0], hs[k][1]);
        st16_sc1(a.ws + WS_XS, fo0, np4(np[0])); st16_sc1(a.ws + WS_XS, fo1, np4(np[1]));
        st16_sc1(a.ws + WS_HS, fo0, np4(hp[0])); st16_sc1(a.ws + WS_HS, fo1, np4(hp[1]));
      }
    }
    wait_lgkm<0>();
    barrier();
    bf16x8 xs[7];
    { const unsigned xr = xsl + lane * 16;
      lds_read128<0>(xs[0], xr); lds_read128<1024>(xs[1], xr); lds_read128<2048>(xs[2], xr); lds_read128<3072>(xs[3], xr);
      lds_read128<4096>(xs[4], xr); lds_read128<5120>(xs[5], xr); lds_read128<6144>(xs[6], xr); }
    f32x4 ge, go, ue, uo;
    asm volatile("s_waitcnt lgkmcnt(6)" : "+v"(xs[0]) :: "memory");
    mfma_new(ge, frag(wa[0][0], wa[0][1]), xs[0]); mfma_new(go, frag(wa[1][0], wa[1][1]), xs[0]);
    mfma_new(ue, frag(wa[14][0], wa[14][1]), xs[0]); mfma_new(uo, frag(wa[15][0], wa[15][1]), xs[0]);
    #define G_STEP(k, n) do { asm volatile("s_waitcnt lgkmcnt(" #n ")" : "+v"(xs[k]) :: "memory"); \
      mfma_acc(ge, frag(wa[2 * k][0], wa[2 * k][1]), xs[k]); mfma_acc(go, frag(wa[2 * k + 1][0], wa[2 * k + 1][1]), xs[k]); \
      mfma_acc(ue, frag(wa[14 + 2 * k][0], wa[14 + 2 * k][1]), xs[k]); mfma_acc(uo, frag(wa[15 + 2 * k][0], wa[15 + 2 * k][1]), xs[k]); } while (0)
    G_STEP(1, 5); G_STEP(2, 4); G_STEP(3, 3); G_STEP(4, 2); G_STEP(5, 1); G_STEP(6, 0);
    #undef G_STEP
    #define FOLD8(e, o) asm volatile("v_add_f32_dpp %0, %4, %0 row_ror:8 row_mask:0xf bank_mask:0xf\n\tv_add_f32_dpp %1, %5, %1 row_ror:8 row_mask:0xf bank_mask:0xf\n\t" \
        "v_add_f32_dpp %2, %6, %2 row_ror:8 row_mask:0xf bank_mask:0xf\n\tv_add_f32_dpp %3, %7, %3 row_ror:8 row_mask:0xf bank_mask:0xf" \
        : "+v"(e[0]), "+v"(e[1]), "+v"(e[2]), "+v"(e[3]) : "v"(o[0]), "v"(o[1]), "v"(o[2]), "v"(o[3]))
    asm volatile("s_nop 7\n\ts_nop 1" : "+v"(ge), "+v"(go), "+v"(ue), "+v"(uo));
    FOLD8(ge, go); FOLD8(ue, uo);
    unsigned gup[2];
    #pragma unroll
    for (int hh = 0; hh < 2; ++hh) {
      const f32x2 g = unpk(cvtpk(ge[2 * hh], ge[2 * hh + 1])), u = unpk(cvtpk(ue[2 * hh], ue[2 * hh + 1]));
      const f32x2 sg = unpk(cvtpk(g[0] * __builtin_amdgcn_rcpf(1.f + __expf(-g[0])), g[1] * __builtin_amdgcn_rcpf(1.f + __expf(-g[1])))) * u;
      gup[hh] = no_poison(cvtpk(sg[0], sg[1]));
    }
    if (cc < 8) st8_sc1(a.ws + WS_GU, gi * 1024 + ((cc + 8 * (w >> 1)) + 16 * (2 * (w & 1) + (n4 >> 1))) * 16 + (n4 & 1) * 8, (v2u){gup[0], gup[1]});
    #pragma unroll
    for (int k = 0; k < 8; ++k) { const unsigned v = key_of(k); asm volatile("s_nop 1\n\tv_writelane_b32 %0, %1, %c2" : "+v"(selkey) : "s"(v), "i"(k)); }
  } else {
    if (isD) {
      wait_vm<0>();
      barrier();
      const unsigned ad = lds0 + w * 14336 + (((8 * g4 + q4) * 32) ^ ((g4 & 1) << 7)) + p4 * 8, adb = lds0 + w * 14336 + (((8 * g4 + 4 + q4) * 32) ^ ((g4 & 1) << 7)) + p4 * 8;
      #define D_RD(kk) tr_read<(kk) * 1024>(wa[kk][0], ad); tr_read<(kk) * 1024>(wa[kk][1], adb);
      D_RD(0) D_RD(1) D_RD(2) D_RD(3) D_RD(4) D_RD(5) D_RD(6) D_RD(7) D_RD(8) D_RD(9) D_RD(10) D_RD(11) D_RD(12) D_RD(13)
      #undef D_RD
      wait_lgkm<0>();
      #pragma unroll
      for (int k = 0; k < 14; ++k) asm volatile("" : "+v"(wa[k][0]), "+v"(wa[k][1]));
    } else {
      for (int i = 0; i < 12; ++i) __builtin_amdgcn_s_sleep(8);
    }
    for (unsigned spins = 0;; ++spins) {
      v2u sel;
      asm volatile("global_load_dwordx2 %0, %1, %2 sc1\n\ts_waitcnt vmcnt(0)" : "=&v"(sel) : "v"((lane & 7) * 128), "s"(a.ws + WS_SEL) : "memory");
      selkey = sel.x;
      if (__all(sel.y == tag) || spins >= SPIN_CAP) break;
      __builtin_amdgcn_s_sleep(4);
    }
    selkey = ((selkey >> 16) << 16) | (2047u - (selkey & 0xffffu));
  }
  {
    const unsigned want = 2047u - (unsigned)(t0 + hl);
    unsigned long long hit = 0;
    #pragma unroll
    for (int k = 0; k < 8; ++k) hit |= __builtin_amdgcn_ballot_w64((__builtin_amdgcn_readlane(selkey, k) & 0xffffu) == want);
    v4u hp[2], np[2]; row_norm(h, mean, rstd, hp, np);
    if (act && !((hit >> lane) & 1)) {
      #pragma unroll
      for (int hf = 0; hf < 2; ++hf) {
        v4u op;
        #pragma unroll
        for (int k = 0; k < 4; ++k) { const f32x2 o = unpk(hp[hf][k]) + unpk(np[hf][k]); op[k] = cvtpk(o[0], o[1]); }
        st16_sc1(a.out, (t0 + hl) * ROWB + hf * 448 + c28 * 16, op);
      }
    }
  }
  if (isD) {
    const unsigned sk = __shfl(selkey, cc & 7);
    unsigned oaddr = (2047u - (sk & 0xffffu)) * ROWB + di * 32 + n4 * 8;
    float gate = rbf(__builtin_amdgcn_rcpf(1.f + __expf(-bfhi(sk))));
    unsigned redw2 = lds0 + 132096 + tid * 16, redr = lds0 + 132096 + lane * 16;
    const unsigned ch = 2 * di + (n4 >> 1), kk = ch >> 2;
    unsigned fo = (kk >> 1) * 1024 + (((cc & 7) + 8 * (kk & 1)) + 16 * (ch & 3)) * 16 + (n4 & 1) * 8, gl = lane * 16;
    asm volatile("" : "+v"(oaddr), "+v"(gate), "+v"(redw2), "+v"(redr), "+v"(fo), "+v"(gl));
    const char* gbp = a.ws + WS_GU + w * 7168;
    v2u hv, nv; v4u gs[7]; f32x4 ye, yo;
    for (unsigned spins = 0;; ++spins) {
      asm volatile("global_load_dwordx4 %0, %9, %10 sc1\n\tglobal_load_dwordx4 %1, %9, %10 offset:1024 sc1\n\tglobal_load_dwordx4 %2, %9, %10 offset:2048 sc1\n\t"
                   "global_load_dwordx4 %3, %9, %10 offset:3072 sc1\n\tglobal_load_dwordx4 %4, %9, %11 sc1\n\tglobal_load_dwordx4 %5, %9, %11 offset:1024 sc1\n\t"
                   "global_load_dwordx4 %6, %9, %11 offset:2048 sc1\n\tglobal_load_dwordx2 %7, %12, %13 sc1\n\tglobal_load_dwordx2 %8, %12, %14 sc1\n\ts_waitcnt vmcnt(0)"
                   : "=&v"(gs[0]), "=&v"(gs[1]), "=&v"(gs[2]), "=&v"(gs[3]), "=&v"(gs[4]), "=&v"(gs[5]), "=&v"(gs[6]), "=&v"(hv), "=&v"(nv)
                   : "v"(gl), "s"(gbp), "s"(gbp + 4096), "v"(fo), "s"(a.ws + WS_HS), "s"(a.ws + WS_XS) : "memory");
      unsigned q0, q1, q2, q3;
      asm volatile("v_pk_max_u16 %0, %4, %5\n\tv_pk_max_u16 %1, %6, %7\n\tv_pk_max_u16 %2, %8, %9\n\tv_pk_max_u16 %3, %10, %11\n\t"
                   "v_pk_max_u16 %0, %0, %1\n\tv_pk_max_u16 %2, %2, %3\n\tv_pk_max_u16 %0, %0, %12\n\tv_pk_max_u16 %0, %0, %2\n\t"
                   "v_lshrrev_b32 %1, 16, %0\n\tv_pk_max_u16 %0, %0, %1"
                   : "=&v"(q0), "=&v"(q1), "=&v"(q2), "=&v"(q3)
                   : "v"(gs[0].w), "v"(gs[1].w), "v"(gs[2].w), "v"(gs[3].w), "v"(gs[4].w), "v"(gs[5].w), "v"(gs[6].w), "v"(hv.y), "v"(nv.y));
      const bool last = spins >= SPIN_CAP;
      if (__builtin_amdgcn_ballot_w64((q0 & 0xffffu) == 0xffffu) != 0 && !last) continue;
      mfma_new(ye, frag(wa[0][0], wa[0][1]), frag(gs[0])); mfma_new(yo, frag(wa[1][0], wa[1][1]), frag(gs[0]));
      #define D_STEP(k) do { asm volatile("v_pk_max_u16 %0, %0, %1\n\tv_pk_max_u16 %0, %0, %2" : "+v"(q0) : "v"(gs[k - 1].x), "v"(gs[k - 1].y)); mfma_acc(ye, frag(wa[2 * k][0], wa[2 * k][1]), frag(gs[k])); \
                             asm volatile("v_pk_max_u16 %0, %0, %1" : "+v"(q0) : "v"(gs[k - 1].z)); mfma_acc(yo, frag(wa[2 * k + 1][0], wa[2 * k + 1][1]), frag(gs[k])); } while (0)
      D_STEP(1); D_STEP(2); D_STEP(3); D_STEP(4); D_STEP(5); D_STEP(6);
      #undef D_STEP
      asm volatile("v_pk_max_u16 %0, %0, %2\n\tv_pk_max_u16 %0, %0, %3\n\tv_pk_max_u16 %0, %0, %4\n\tv_pk_max_u16 %0, %0, %5\n\tv_pk_max_u16 %0, %0, %6\n\t"
                   "v_lshrrev_b32 %1, 16, %0\n\tv_pk_max_u16 %0, %0, %1\n\ts_nop 2"
                   : "+v"(q0), "+v"(q1) : "v"(gs[6].x), "v"(gs[6].y), "v"(gs[6].z), "v"(hv.x), "v"(nv.x));
      if (__builtin_amdgcn_ballot_w64((q0 & 0xffffu) == 0xffffu) == 0 || last) break;
    }
    asm volatile("s_nop 1\n\tv_add_f32_dpp %0, %4, %0 row_ror:8 row_mask:0xf bank_mask:0xf\n\tv_add_f32_dpp %1, %5, %1 row_ror:8 row_mask:0xf bank_mask:0xf\n\t"
                 "v_add_f32_dpp %2, %6, %2 row_ror:8 row_mask:0xf bank_mask:0xf\n\tv_add_f32_dpp %3, %7, %3 row_ror:8 row_mask:0xf bank_mask:0xf"
                 : "+v"(ye[0]), "+v"(ye[1]), "+v"(ye[2]), "+v"(ye[3]) : "v"(yo[0]), "v"(yo[1]), "v"(yo[2]), "v"(yo[3]));
    f32x4 r0, r1, r2, r3;
    asm volatile("ds_write_b128 %4, %5\n\ts_waitcnt lgkmcnt(0)\n\ts_barrier\n\tds_read_b128 %0, %6\n\tds_read_b128 %1, %6 offset:1024\n\tds_read_b128 %2, %6 offset:2048\n\t"
                 "ds_read_b128 %3, %6 offset:3072\n\ts_waitcnt lgkmcnt(0)"
                 : "=&v"(r0), "=&v"(r1), "=&v"(r2), "=&v"(r3) : "v"(redw2), "v"(ye), "v"(redr) : "memory");
    if (w == 0 && cc < 8) {
      const f32x4 t = (r0 + r1) + (r2 + r3);
      unsigned op[2];
      #pragma unroll
      for (int hh = 0; hh < 2; ++hh) {
        const f32x2 y1 = unpk(cvtpk(t[2 * hh], t[2 * hh + 1])) * gate, y2 = unpk(cvtpk(y1[0], y1[1])) + unpk(hh ? nv.y : nv.x);
        const f32x2 o = unpk(cvtpk(y2[0], y2[1])) + unpk(hh ? hv.y : hv.x);
        op[hh] = cvtpk(o[0], o[1]);
      }
      st8_sc1(a.out, oaddr, (v2u){op[0], op[1]});
    }
  }
}

extern "C" void kernel_launch(void* const* d_in, const int* in_sizes, int n_in, void* d_out, int out_size, void* d_ws, size_t ws_size, hipStream_t stream) {
  Args a;
  a.x = (const u16*)d_in[0]; a.cre = (const u16*)d_in[1]; a.cim = (const u16*)d_in[2]; a.wre = (const u16*)d_in[3]; a.wim = (const u16*)d_in[4];
  a.wr = (const u16*)d_in[6]; a.wg = (const u16*)d_in[7]; a.wu = (const u16*)d_in[8]; a.wd = (const u16*)d_in[9];
  a.out = (u16*)d_out; a.ws = (char*)d_ws;
  (void)hipFuncSetAttribute((const void*)fnet_moe_kernel, hipFuncAttributeMaxDynamicSharedMemorySize, LDS_BYTES);
  fnet_moe_kernel<<<256, 256, LDS_BYTES, stream>>>(a);
}
```
